```python
import torch

def _softmax(x, axis=-1):
    return torch.softmax(x, dim=axis)
def _gelu(x):
    return torch.nn.functional.gelu(x, approximate="tanh")
def _silu(x):
    return torch.nn.functional.silu(x)
def _sigmoid(x):
    return torch.sigmoid(x)
def _layernorm(x, axis=-1, eps=1e-5):
    m = x.mean(dim=axis, keepdim=True)
    v = x.var(dim=axis, keepdim=True, unbiased=False)
    return (x - m) / torch.sqrt(v + eps)
def _scatter_add(tgt, idx, src, axis=0):
    return tgt.clone().index_add_(axis, idx, src)
def _segment_sum(src, seg, n):
    out = torch.zeros((n,) + src.shape[1:], dtype=src.dtype)
    return out.index_add_(0, seg, src)
def _pad(x, pw):
    flat = tuple(v for pair in reversed(pw) for v in pair)
    return torch.nn.functional.pad(x, flat)

kernel_name = 'gated_mix__sliding_block_cached__dense_activation_gelu__decode'

def setup_inputs():

    _w = (lambda a: (lambda t: t.to(torch.bfloat16) if t.is_floating_point() else t)(torch.as_tensor(a)))
    def _A_setup():
        B, Sq, D = 1, 1, 512
        Hq, Hkv, Sk, Dh = 32, 8, 65536, 128
        assert Hq % Hkv == 0, (Hq, Hkv)
        return {
            "x": _w((lambda *s: torch.randn(*s, dtype=torch.bfloat16) * 0.05)(B, Sq, D)),
            "k_cache": _w((lambda *s: torch.randn(*s, dtype=torch.bfloat16) * 3.2)(B, Hkv, Sk, Dh)),
            "v_cache": _w((lambda *s: torch.randn(*s, dtype=torch.bfloat16) * 0.4)(B, Hkv, Sk, Dh)),
            "wq": _w((lambda *s: torch.randn(*s, dtype=torch.bfloat16) * 3.2)(D, Hq * Dh) * (D ** -0.5)),
            "wo": _w((lambda *s: torch.randn(*s, dtype=torch.bfloat16) * 0.4)(Hq * Dh, D) * ((Hq * Dh) ** -0.5)),
        }
    _A_in = _A_setup()

    def _B_setup():
        B, S, D, F = 1, 1, 512, 1792
        return {
            "x": _w((lambda *s: torch.randn(*s, dtype=torch.bfloat16) * 0.05)(B, S, D)),
            "w1": _w((lambda *s: torch.randn(*s, dtype=torch.bfloat16) * 0.2)(D, F) * (D ** -0.5)),
            "b1": _w((lambda *s: torch.randn(*s, dtype=torch.bfloat16) * 0.2)(F) * 0.01),
            "w2": _w((lambda *s: torch.randn(*s, dtype=torch.bfloat16) * 0.2)(F, D) * (F ** -0.5)),
            "b2": _w((lambda *s: torch.randn(*s, dtype=torch.bfloat16) * 0.05)(D) * 0.01),
        }
    _B_in = _B_setup()

    return {
        "A_x": _A_in["x"],
        "A_k_cache": _A_in["k_cache"],
        "A_v_cache": _A_in["v_cache"],
        "A_wq": _A_in["wq"],
        "A_wo": _A_in["wo"],
        "B_x": _B_in["x"],
        "B_w1": _B_in["w1"],
        "B_b1": _B_in["b1"],
        "B_w2": _B_in["w2"],
        "B_b2": _B_in["b2"],
        "gate_w": _w((lambda *s: torch.randn(*s, dtype=torch.bfloat16) * 0.1)(512) * 16.0),
    }

def reference(A_x, A_k_cache, A_v_cache, A_wq, A_wo, B_x, B_w1, B_b1, B_w2, B_b2, gate_w):
    def _A_ref(x, k_cache, v_cache, wq, wo):
        B, Sq, D = x.shape
        Hq, Dh = 32, 128
        Hkv, Sk = k_cache.shape[1], k_cache.shape[2]
        W = min(4096, max(256, Sk // 4))
        rep = Hq // Hkv
        q = (lambda x, *d: x.permute(*d))((x @ wq).reshape(B, Sq, Hq, Dh), 0, 2, 1, 3)
        k = (lambda x, n, axis: torch.repeat_interleave(x, n, dim=axis))(k_cache[:, :, Sk - W:, :], rep, axis=1)
        v = (lambda x, n, axis: torch.repeat_interleave(x, n, dim=axis))(v_cache[:, :, Sk - W:, :], rep, axis=1)
        scores = torch.einsum("bhqd,bhkd->bhqk", q, k) * (Dh ** -0.5)
        p = _softmax(scores, axis=-1)
        o = torch.einsum("bhqk,bhkd->bhqd", p, v)
        return (lambda x, *d: x.permute(*d))(o, 0, 2, 1, 3).reshape(B, Sq, Hq * Dh) @ wo

    def _B_ref(x, w1, b1, w2, b2):
        h = torch.einsum("bsd,df->bsf", x, w1) + b1
        h = _gelu(h)
        return torch.einsum("bsf,fd->bsd", h, w2) + b2

    x = A_x
    g = _sigmoid((x * gate_w).sum(-1, keepdims=True))
    a = _A_ref(x, A_k_cache, A_v_cache, A_wq, A_wo)
    b = _B_ref(x, B_w1, B_b1, B_w2, B_b2)
    return g * a + (1 - g) * b
```

```cpp
#include <hip/hip_runtime.h>
#include <hip/hip_bf16.h>
#include <stdint.h>

typedef unsigned short u16;
__device__ __forceinline__ float bf2f(u16 v) { return __uint_as_float(((unsigned)v) << 16); }
__device__ __forceinline__ u16 f2bf(float f) { __bf16 b = (__bf16)f; return __builtin_bit_cast(u16, b); }

constexpr int D = 512, HQ = 32, HKV = 8, SK = 65536, DH = 128, W = 4096, F = 1792;
constexpr int NS = 32;

__global__ void k1(const u16* x, const u16* wq, const u16* w1, const u16* b1, float* q, float* h) {
    int c = blockIdx.x * blockDim.x + threadIdx.x;
    if (c < HQ * DH) {
        float s = 0.f;
        for (int i = 0; i < D; ++i) s += bf2f(x[i]) * bf2f(wq[(size_t)i * (HQ * DH) + c]);
        q[c] = bf2f(f2bf(s));
    } else if (c < HQ * DH + F) {
        int f = c - HQ * DH;
        float s = 0.f;
        for (int i = 0; i < D; ++i) s += bf2f(x[i]) * bf2f(w1[(size_t)i * F + f]);
        s += bf2f(b1[f]);
        float t = tanhf(0.7978845608028654f * (s + 0.044715f * s * s * s));
        h[f] = 0.5f * s * (1.f + t);
    }
}
__global__ void k2(const u16* k, const u16* v, const float* q, float* po, float* pm, float* pl) {
    __shared__ float sp[4][128];
    __shared__ float red[4][2];
    int s = blockIdx.x, g = blockIdx.y, t = threadIdx.x;
    const u16* kb = k + ((size_t)g * SK + (SK - W) + s * 128) * DH;
    const u16* vb = v + ((size_t)g * SK + (SK - W) + s * 128) * DH;
    float sc[4] = {0, 0, 0, 0};
    for (int d = 0; d < DH; ++d) { float kv = bf2f(kb[(size_t)t * DH + d]);
        for (int h = 0; h < 4; ++h) sc[h] += kv * q[(g * 4 + h) * DH + d]; }
    for (int h = 0; h < 4; ++h) sp[h][t] = sc[h] * 0.08838834764831845f;
    __syncthreads();
    if (t < 4) { float m = -INFINITY; for (int j = 0; j < 128; ++j) m = fmaxf(m, sp[t][j]);
        float l = 0; for (int j = 0; j < 128; ++j) { float p = __expf(sp[t][j] - m); sp[t][j] = p; l += p; }
        red[t][0] = m; red[t][1] = l; }
    __syncthreads();
    float o[4] = {0, 0, 0, 0};
    for (int j = 0; j < 128; ++j) { float vv = bf2f(vb[(size_t)j * DH + t]);
        for (int h = 0; h < 4; ++h) o[h] += sp[h][j] * vv; }
    for (int h = 0; h < 4; ++h) po[((g * 4 + h) * NS + s) * DH + t] = o[h];
    if (t < 4) { pm[(g * 4 + t) * NS + s] = red[t][0]; pl[(g * 4 + t) * NS + s] = red[t][1]; }
}
__global__ void k3(const float* po, const float* pm, const float* pl, float* o) {
    int h = blockIdx.x, t = threadIdx.x;
    float M = -INFINITY; for (int s = 0; s < NS; ++s) M = fmaxf(M, pm[h * NS + s]);
    float L = 0, a = 0; for (int s = 0; s < NS; ++s) { float w = __expf(pm[h * NS + s] - M); L += w * pl[h * NS + s]; a += w * po[(h * NS + s) * DH + t]; }
    o[h * DH + t] = bf2f(f2bf(a / L));
}
__global__ void k4(const u16* x, const u16* gw, const float* o, const float* h, const u16* wo, const u16* w2, const u16* b2, u16* out) {
    __shared__ float ra[256], rb[256], rg[256];
    int c = blockIdx.x, t = threadIdx.x;
    float a = 0, b = 0, g = 0;
    for (int i = t; i < HQ * DH; i += 256) a += o[i] * bf2f(wo[(size_t)i * D + c]);
    for (int i = t; i < F; i += 256) b += h[i] * bf2f(w2[(size_t)i * D + c]);
    for (int i = t; i < D; i += 256) g += bf2f(x[i]) * bf2f(gw[i]);
    ra[t] = a; rb[t] = b; rg[t] = g; __syncthreads();
    for (int s = 128; s > 0; s >>= 1) { if (t < s) { ra[t] += ra[t + s]; rb[t] += rb[t + s]; rg[t] += rg[t + s]; } __syncthreads(); }
    if (t == 0) { float gg = 1.f / (1.f + __expf(-rg[0])); float bb = rb[0] + bf2f(b2[c]); out[c] = f2bf(gg * ra[0] + (1.f - gg) * bb); }
}

extern "C" void kernel_launch(void* const* d_in, const int* in_sizes, int n_in, void* d_out, int out_size, void* d_ws, size_t ws_size, hipStream_t stream) {
    const u16* x = (const u16*)d_in[0]; const u16* k = (const u16*)d_in[1]; const u16* v = (const u16*)d_in[2];
    const u16* wq = (const u16*)d_in[3]; const u16* wo = (const u16*)d_in[4];
    const u16* w1 = (const u16*)d_in[6]; const u16* b1 = (const u16*)d_in[7]; const u16* w2 = (const u16*)d_in[8]; const u16* b2 = (const u16*)d_in[9];
    const u16* gw = (const u16*)d_in[10];
    float* ws = (float*)d_ws;
    float* q = ws; float* h = q + 4096; float* o = h + 2048; float* pm = o + 4096; float* pl = pm + 1024; float* po = pl + 1024;
    k1<<<(4096 + 1792 + 63) / 64, 64, 0, stream>>>(x, wq, w1, b1, q, h);
    k2<<<dim3(NS, HKV), 128, 0, stream>>>(k, v, q, po, pm, pl);
    k3<<<32, 128, 0, stream>>>(po, pm, pl, o);
    k4<<<512, 256, 0, stream>>>(x, gw, o, h, wo, w2, b2, (u16*)d_out);
}
```

```cpp
#include <hip/hip_runtime.h>
#include <stdint.h>

typedef unsigned u32;
typedef unsigned long long u64;
typedef u32 u32x4 __attribute__((ext_vector_type(4)));
typedef float f32x4 __attribute__((ext_vector_type(4)));
typedef __bf16 bf16x8 __attribute__((ext_vector_type(8)));
typedef __bf16 bf16x2 __attribute__((ext_vector_type(2)));
#define GAS __attribute__((address_space(1)))
#define LAS __attribute__((address_space(3)))
#define SB() __builtin_amdgcn_sched_barrier(0)
#define MEMBAR() asm volatile("" ::: "memory")

#define GATHER_AT 9
#define VHOLD 0
#define DELAY_ML 8
#define DELAY_O 8
#define DELAY_Y 8
#define WO_FIRST 0
#define DELAY_F 8
#define F_TRIG 4
#define K0 32
#define REL_F 25
#define REL_T 9
#define VWIN 32
#define PRIME 0
#define PREW 0
#define WSOFF 0

constexpr int SK = 65536, WIN = 4096;
constexpr u32 SPIN_CAP = 1u << 16;
constexpr u32 WS_EPOCH = 0;
constexpr u32 WS_Q = 4096;
constexpr u32 WS_ML = WS_Q + 8 * 16384;
constexpr u32 WS_F = WS_ML + 32768;
constexpr u32 WS_O = WS_F + 8 * 896 * 16;
constexpr u32 WS_Y = WS_O + (1u << 19);
constexpr u32 WS_END = WS_Y + (1u << 18);
constexpr u32 WS_STAMP = 4u << 20;
constexpr int L_FLAG = 0;
constexpr int L_DUMMY = 48;
constexpr int L_A = 64;
constexpr int L_W = 384;
constexpr int L_Z = 128;
constexpr int L_Q = 512;
constexpr int L_P = L_Q + 1280;
constexpr int L_END = L_P + 1280;

struct Args {
    const char* x; const char* k; const char* v; const char* wq; const char* wo;
    const char* w1; const char* b1; const char* w2; const char* b2; const char* gw;
    char* out; char* ws;
};

#define ST(i)
#define CNT(i, n)
#define STF(i, n)
#define ST_DECL
#define ST_OUT()

__device__ __forceinline__ u32x4 ldnt(const char* p) { return __builtin_nontemporal_load((const GAS u32x4*)p); }
typedef __amdgpu_buffer_rsrc_t rsrc_t;
__device__ __forceinline__ rsrc_t mk(const char* p, u32 n) { return __builtin_amdgcn_make_buffer_rsrc((void*)p, 0, n, 0x00020000); }
__device__ __forceinline__ u32x4 bnt(rsrc_t r, u32 v, u32 s) { return __builtin_amdgcn_raw_buffer_load_b128(r, v, s, 2); }
__device__ __forceinline__ u32x4 bld(rsrc_t r, u32 v, u32 s) { return __builtin_amdgcn_raw_buffer_load_b128(r, v, s, 16); }
__device__ __forceinline__ void bstu(u32 a, u32 b, u32 tag, rsrc_t r, u32 v, u32 s) { __builtin_amdgcn_raw_buffer_store_b128(u32x4{a, tag, b, tag}, r, v, s, 16); }
__device__ __forceinline__ u32 chk(u32 bad, u32x4 x, u32 tag) { bad |= x[1] ^ tag; asm volatile("" : "+v"(bad)); bad |= x[3] ^ tag; asm volatile("" : "+v"(bad)); return bad; }
__device__ __forceinline__ void bst(float a, float b, u32 tag, rsrc_t r, u32 v, u32 s) { __builtin_amdgcn_raw_buffer_store_b128(u32x4{__float_as_uint(a), tag, __float_as_uint(b), tag}, r, v, s, 16); }
#define PIN(x) asm volatile("" : "+v"(x))
constexpr u32 OOB = 0x80000000u;
constexpr u32 CODE_BYTES = 12800;
__device__ __forceinline__ float dot2(u32 a, u32 b, float c) { return __builtin_amdgcn_fdot2_f32_bf16(__builtin_bit_cast(bf16x2, a), __builtin_bit_cast(bf16x2, b), c, false); }
__device__ __forceinline__ u32 cvtpk(float lo, float hi) { bf16x2 r = {(__bf16)lo, (__bf16)hi}; return __builtin_bit_cast(u32, r); }
template <int CTRL> __device__ __forceinline__ float dpp(float x) { return __builtin_bit_cast(float, __builtin_amdgcn_mov_dpp(__builtin_bit_cast(int, x), CTRL, 0xf, 0xf, true)); }
constexpr int XOR1 = 0xB1, XOR2 = 0x4E, ROR4 = 0x124, ROR8 = 0x128;
__device__ __forceinline__ float sw16_add(float a, float b) { auto s = __builtin_amdgcn_permlane16_swap(__float_as_uint(a), __float_as_uint(b), false, false); return __uint_as_float(s[0]) + __uint_as_float(s[1]); }
__device__ __forceinline__ float sw32_add(float a, float b) { auto s = __builtin_amdgcn_permlane32_swap(__float_as_uint(a), __float_as_uint(b), false, false); return __uint_as_float(s[0]) + __uint_as_float(s[1]); }
__device__ __forceinline__ float fmx(float a, float b) { return __builtin_elementwise_maximum(a, b); }
__device__ __forceinline__ float sw16_max(float a) { auto s = __builtin_amdgcn_permlane16_swap(__float_as_uint(a), __float_as_uint(a), false, false); return fmx(__uint_as_float(s[0]), __uint_as_float(s[1])); }
__device__ __forceinline__ float sw32_max(float a) { auto s = __builtin_amdgcn_permlane32_swap(__float_as_uint(a), __float_as_uint(a), false, false); return fmx(__uint_as_float(s[0]), __uint_as_float(s[1])); }
__device__ __forceinline__ f32x4 mfma(u32x4 a, u32x4 b, f32x4 c) { return __builtin_amdgcn_mfma_f32_16x16x32_bf16(__builtin_bit_cast(bf16x8, a), __builtin_bit_cast(bf16x8, b), c, 0, 0, 0); }
__device__ __forceinline__ float bf_lo(u32 w) { return __uint_as_float(w << 16); }
__device__ __forceinline__ float bf_hi(u32 w) { return __uint_as_float(w & 0xffff0000u); }

__device__ __forceinline__ void transpose8(const u32x4* r, u32x4* t) {
#pragma unroll
    for (int p = 0; p < 4; ++p)
#pragma unroll
        for (int d = 0; d < 4; ++d) {
            t[2 * d][p] = __builtin_amdgcn_perm(r[2 * p + 1][d], r[2 * p][d], 0x05040100u);
            t[2 * d + 1][p] = __builtin_amdgcn_perm(r[2 * p + 1][d], r[2 * p][d], 0x07060302u);
        }
}
__device__ __forceinline__ void row_fma(float* acc, u32x4 w, u32 xpair, int odd) {
    const u32 xl = odd ? xpair >> 16 : xpair & 0xffffu, xh = odd ? xpair & 0xffff0000u : xpair << 16;
#pragma unroll
    for (int d = 0; d < 4; ++d) { acc[2 * d] = dot2(w[d], xl, acc[2 * d]); acc[2 * d + 1] = dot2(w[d], xh, acc[2 * d + 1]); }
}
__device__ __forceinline__ void reduce8(const float* acc, float& u0, float& u1) {
    const float s0 = sw32_add(acc[0], acc[4]), s1 = sw32_add(acc[1], acc[5]), s2 = sw32_add(acc[2], acc[6]), s3 = sw32_add(acc[3], acc[7]);
    u0 = sw16_add(s0, s2); u1 = sw16_add(s1, s3);
    u0 += dpp<ROR8>(u0); u1 += dpp<ROR8>(u1);
}
__device__ __forceinline__ float gate_of(u32x4 x, u32x4 gw) {
    float s = 0.f;
#pragma unroll
    for (int d = 0; d < 4; ++d) s = dot2(x[d], gw[d], s);
    s += dpp<XOR1>(s); s += dpp<XOR2>(s); s += dpp<ROR4>(s); s += dpp<ROR8>(s);
    s = sw16_add(s, s); s = sw32_add(s, s);
    return __builtin_amdgcn_rcpf(1.f + __builtin_amdgcn_exp2f(-1.4426950408889634f * s));
}
template <int OFF> __device__ __forceinline__ void lds_wait() {
    u32 t, n, c;
    asm volatile("s_mov_b32 %1, 0x100000\n\tv_mov_b32 %0, 0\n"
                 "1:\n\tds_read_b32 %0, %0 offset:%3\n\ts_waitcnt lgkmcnt(0)\n\tv_readfirstlane_b32 %2, %0\n\ts_cmp_lg_u32 %2, 0\n\ts_cbranch_scc1 2f\n\t"
                 "s_sleep 1\n\ts_sub_u32 %1, %1, 1\n\ts_cbranch_scc0 1b\n"
                 "2:" : "=&v"(t), "=&s"(n), "=&s"(c) : "n"(OFF) : "memory", "scc");
}
#define BARRIER() do { asm volatile("s_waitcnt lgkmcnt(0)" ::: "memory"); __builtin_amdgcn_s_barrier(); MEMBAR(); } while (0)

__global__ void __launch_bounds__(256, 2) gated_mix_kernel(Args a) {
    __shared__ __attribute__((aligned(16))) char lds_[L_END];
    LAS char* lds = (LAS char*)lds_;
    volatile LAS u32* flag = (volatile LAS u32*)(lds + L_FLAG);
    const int b = blockIdx.x, g = b & 7, s = b >> 3, lane = threadIdx.x & 63;
    int wv = __builtin_amdgcn_readfirstlane(threadIdx.x >> 6);
    ST_DECL; ST(0);
    u64 pc; asm volatile("s_getpc_b64 %0" : "=s"(pc));
    if (threadIdx.x < L_Q / 4) *(volatile LAS u32*)(lds + threadIdx.x * 4) = 0;
    asm volatile("" :: "s"(a.x), "s"(a.k), "s"(a.v), "s"(a.wq), "s"(a.wo), "s"(a.w1), "s"(a.b1), "s"(a.w2), "s"(a.b2), "s"(a.gw), "s"(a.out), "s"(a.ws));
    BARRIER();
    const rsrc_t ws = mk(a.ws, WS_END);
    const u32 kvoff = (u32)(g * SK + (SK - WIN) + s * 128) * 256u;
    u32 cw = __builtin_amdgcn_raw_buffer_load_b32(mk((const char*)(pc & ~255ull), wv == 3 ? CODE_BYTES : 0), (lane & 15) * 128 + (lane >> 4) * 8192, (s & 3) * 2048, 0);
    SB();
    const int rho = 8 * g + (s >> 2), cq = s & 3;

    if (wv == 0) {
        const int kq = s & 3, r8 = lane >> 3;
        u32 tag = __builtin_amdgcn_raw_buffer_load_b32(ws, 0, WS_EPOCH, 16);
        u32x4 xr[2], w[16], kf[32];
        const char* xp = a.x + (kq * 128 + r8 * 16) * 2;
        xr[0] = *(const GAS u32x4*)xp; xr[1] = *(const GAS u32x4*)(xp + 16);
        const rsrc_t rq = mk(a.wq + kq * (128 * 8192) + g * 1024 + (s >> 2) * 128, 128 * 8192);
        const u32 wo_ = r8 * (16 * 8192) + (lane & 7) * 16;
#pragma unroll
        for (int i = 0; i < 16; ++i) { w[i] = bnt(rq, wo_, i * 8192); SB(); }
        const int key3 = (lane >> 1) & 7;
        const u32 ko = key3 * 256 + (2 * (lane >> 4) + (lane & 1)) * 16;
        const rsrc_t rk = mk(a.k + kvoff, 32768);
#pragma unroll
        for (int i = 0; i < K0; ++i) kf[i] = bnt(rk, ko + ((i >> 1) & 1) * 2048 + (i & 1) * 128, (i >> 2) * 4096);
        SB();
        tag += 1;
        flag[10] = tag;
        ST(1);
        const int hd = (lane >> 2) & 3, xb = (lane >> 1) & 1, yb = lane & 1;
        u32 qsa = (lane & 8) ? OOB : ((s >> 2) * 32 + 4 * (lane & 7) + 2 * (lane >> 5) + ((lane >> 4) & 1)) * 16;
        u32 qa0 = L_Q + hd * 320 + (2 * (lane >> 4) + yb) * 16, qa1 = xb ? qa0 : L_Z;
        qa0 = xb ? L_Z : qa0;
        u32 pwa = L_P + hd * 320 + (2 * xb + (lane >> 5)) * 64 + (2 * ((lane >> 4) & 1) + yb) * 16;
        u32 mla = (lane & 0x23) ? OOB : hd * 16 + (lane & 16) * 4;
        u32 l16 = lane * 16;
        PIN(qsa); PIN(qa0); PIN(qa1); PIN(pwa); PIN(mla); PIN(l16);
        SB();
        float acc[8] = {0.f, 0.f, 0.f, 0.f, 0.f, 0.f, 0.f, 0.f};
#pragma unroll
        for (int i = 0; i < 16; ++i) row_fma(acc, w[i], xr[i >> 3][(i >> 1) & 3], i & 1);
        float u0, u1;
        reduce8(acc, u0, u1);
        ST(2);
        bst(u0, u1, tag, ws, qsa, WS_Q + g * 16384 + kq * 4096);
        SB();
#pragma unroll
        for (int i = K0; i < 32; ++i) {
            if ((i & 3) == 0) { asm volatile("s_waitcnt vmcnt(%0)" :: "n"(K0 - 3) : "memory"); SB(); }
            kf[i] = bnt(rk, ko + ((i >> 1) & 1) * 2048 + (i & 1) * 128, (i >> 2) * 4096);
            SB();
        }
        asm volatile("s_waitcnt vmcnt(%0)" :: "n"(REL_F) : "memory");
        flag[0] = 1;
        asm volatile("s_waitcnt vmcnt(%0)" :: "n"(GATHER_AT) : "memory");
        ST(3);
        u32x4 qg[16];
#pragma nounroll
        for (u32 n = 0; n < SPIN_CAP; ++n) {
            MEMBAR();
#pragma unroll
            for (int i = 0; i < 16; ++i) qg[i] = bld(ws, l16 + (i >> 2) * 1024, WS_Q + g * 16384 + (i & 3) * 4096);
            SB();
            flag[3] = 1;
            SB();
            u32 bad = 0;
#pragma unroll
            for (int i = 0; i < 16; ++i) bad = chk(bad, qg[i], tag);
            CNT(8, n); STF(7, n);
            if (__builtin_amdgcn_ballot_w64(bad != 0) == 0) break;
        }
        ST(4);
#pragma unroll
        for (int t = 0; t < 4; ++t) {
            const float q0 = (__uint_as_float(qg[4 * t][0]) + __uint_as_float(qg[4 * t + 1][0])) + (__uint_as_float(qg[4 * t + 2][0]) + __uint_as_float(qg[4 * t + 3][0]));
            const float q1 = (__uint_as_float(qg[4 * t][2]) + __uint_as_float(qg[4 * t + 1][2])) + (__uint_as_float(qg[4 * t + 2][2]) + __uint_as_float(qg[4 * t + 3][2]));
            *(volatile LAS u32*)(lds + L_Q + t * 320 + lane * 4) = cvtpk(q0, q1);
        }
        u32x4 qf[2][2];
        qf[0][0] = *(volatile LAS u32x4*)(lds + qa0); qf[0][1] = *(volatile LAS u32x4*)(lds + qa0 + 128);
        qf[1][0] = *(volatile LAS u32x4*)(lds + qa1); qf[1][1] = *(volatile LAS u32x4*)(lds + qa1 + 128);
        SB();
        float sc[8];
#pragma unroll
        for (int n = 0; n < 8; ++n) {
            f32x4 d = {0.f, 0.f, 0.f, 0.f};
            d = mfma(kf[4 * n], qf[0][0], d); d = mfma(kf[4 * n + 1], qf[0][1], d);
            d = mfma(kf[4 * n + 2], qf[1][0], d); d = mfma(kf[4 * n + 3], qf[1][1], d);
            const float p = yb ? d[3] : d[0], q = yb ? d[1] : d[2];
            sc[n] = p + dpp<XOR1>(q);
        }
        ST(5);
        constexpr float C2 = 0.08838834764831845f * 1.4426950408889634f;
        float m = fmx(fmx(fmx(sc[0], sc[1]), fmx(sc[2], sc[3])), fmx(fmx(sc[4], sc[5]), fmx(sc[6], sc[7])));
        m = fmx(m, dpp<XOR1>(m)); m = fmx(m, dpp<XOR2>(m)); m = sw16_max(m); m = sw32_max(m);
        m *= C2;
        float p[8];
#pragma unroll
        for (int n = 0; n < 8; ++n) p[n] = __builtin_amdgcn_exp2f(fmaf(sc[n], C2, -m));
        *(volatile LAS u32x4*)(lds + pwa) = u32x4{cvtpk(p[0], p[1]), cvtpk(p[2], p[3]), cvtpk(p[4], p[5]), cvtpk(p[6], p[7])};
        flag[5] = 1;
        SB();
        ST(6);
        float l = ((p[0] + p[1]) + (p[2] + p[3])) + ((p[4] + p[5]) + (p[6] + p[7]));
        l += dpp<XOR1>(l); l += dpp<XOR2>(l); l = sw16_add(l, l); l = sw32_add(l, l);
        bst(m, l, tag, ws, mla, WS_ML + g * 4096 + s * 128);
        ST_OUT();
    }
    asm volatile("" : "+s"(wv));
    if (wv == 1) {
        u32 oa = ((lane >> 4) * 2 + ((lane >> 3) & 1)) * 8192 + (lane & 7) * 16;
        u32 pa = L_P + ((lane >> 2) & 3) * 320 + (lane >> 4) * 16;
        u32 l16 = lane * 16;
        PIN(oa); PIN(pa); PIN(l16);
        const rsrc_t rv = mk(a.v + kvoff, 32768);
        if (PREW & 2) { bst(0.f, 0.f, 0, ws, oa, WS_O + g * 65536 + s * 256); bst(0.f, 0.f, 0, ws, oa + 128, WS_O + g * 65536 + s * 256); }
        lds_wait<12>();
        ST(1);
        u32x4 v[32];
#pragma unroll
        for (int i = 0; i < 32; ++i) {
            if (i >= VWIN && (i & 3) == 0) { SB(); asm volatile("s_waitcnt vmcnt(%0)" :: "n"(VWIN - 4) : "memory"); SB(); }
            v[i] = bnt(rv, l16 + (i >> 3) * 1024, (i & 7) * 4096);
        }
        SB();
        flag[4] = 1;
        u32 tag = flag[10]; PIN(tag);
        ST(2);
        u32x4 t0[8], t1[8], t2[8], t3[8], pf[4];
        transpose8(v, t0);
        transpose8(v + 8, t1);
        transpose8(v + 16, t2);
#pragma unroll
        for (int i = 0; i < 8; ++i) { PIN(t0[i]); PIN(t1[i]); PIN(t2[i]); }
        SB();
#pragma nounroll
        for (u32 n = 0; n < SPIN_CAP * 16; ++n) {
            const u32 f = flag[5];
#pragma unroll
            for (int i = 0; i < 4; ++i) pf[i] = *(volatile LAS u32x4*)(lds + pa + i * 64);
            if (__builtin_amdgcn_readfirstlane(f)) break;
        }
        ST(3);
        f32x4 o[8];
#pragma unroll
        for (int e = 0; e < 8; ++e) { o[e] = mfma(pf[0], t0[e], f32x4{0.f, 0.f, 0.f, 0.f}); }
#pragma unroll
        for (int e = 0; e < 8; ++e) o[e] = mfma(pf[1], t1[e], o[e]);
#pragma unroll
        for (int e = 0; e < 8; ++e) o[e] = mfma(pf[2], t2[e], o[e]);
        transpose8(v + 24, t3);
        ST(4);
#pragma unroll
        for (int e = 0; e < 8; ++e) o[e] = mfma(pf[3], t3[e], o[e]);
#pragma unroll
        for (int k = 0; k < 2; ++k) bstu(cvtpk(o[4 * k][0], o[4 * k + 1][0]), cvtpk(o[4 * k + 2][0], o[4 * k + 3][0]), tag, ws, oa + k * 128, WS_O + g * 65536 + s * 256);
        flag[6] = 1;
        ST_OUT();
    }
    asm volatile("" : "+s"(wv));
    if (wv == 2) {
        const int bb = b < 224 ? b : b - 224, kgrp = bb / 28, cgrp = bb - kgrp * 28, r8 = lane >> 3;
        const int j = g * 32 + s, col = 128 * (j >> 6) + 8 * (j & 15) + 2 * ((j >> 4) & 3);
        const u32x4 xa = *(const GAS u32x4*)(a.x + lane * 16), gw = *(const GAS u32x4*)(a.gw + lane * 16);
        const u32x4 xr = *(const GAS u32x4*)(a.x + (kgrp * 64 + r8 * 8) * 2);
        const int pr = (lane & 15) < 14 ? (lane & 15) : 13;
        const u32 b1w = *(const GAS u32*)(a.b1 + (rho * 14 + pr) * 4);
        const u32 b2w = __builtin_amdgcn_raw_buffer_load_b32(mk(a.b2, 1024), 0, col * 2, 0);
        SB();
        const rsrc_t r1 = mk(a.w1 + kgrp * (64 * 3584) + cgrp * 128, b < 224 ? 64 * 3584 - cgrp * 128 : 0);
        u32 wa = r8 * (8 * 3584) + (lane & 7) * 16;
        u32 fsa = ((lane & 8) || b >= 224) ? OOB : (cgrp * 32 + 4 * (lane & 7) + 2 * (lane >> 5) + ((lane >> 4) & 1)) * 16;
        u32 fa = (lane >> 4) * 14336 + (rho * 14 + pr) * 16;
        u32 ha = lane < 14 ? L_A + lane * 4 : L_DUMMY;
        u32 ya = lane * 1024, outa = (lane & 31) ? OOB : (lane >> 4), epa = (b | lane) ? OOB : 0;
        u32 mla = (lane >> 4) * 1024, wla = L_W + (lane >> 4) * 16;
        PIN(mla); PIN(wla);
        PIN(wa); PIN(fsa); PIN(fa); PIN(ha); PIN(ya); PIN(outa); PIN(epa);
        lds_wait<0>();
        u32x4 w[8];
#pragma unroll
        for (int i = 0; i < 8; ++i) w[i] = bnt(r1, wa, i * 3584);
        SB();
        ST(2);
        float g1 = 1.f - gate_of(xa, gw), bb0 = bf_lo(b1w), bb1 = bf_hi(b1w);
        float bias = g1 * (lane < 32 ? bf_lo(b2w) : bf_hi(b2w));
        u32 tag = flag[10];
        PIN(g1); PIN(bb0); PIN(bb1); PIN(bias); PIN(tag);
        SB();
        float acc[8] = {0.f, 0.f, 0.f, 0.f, 0.f, 0.f, 0.f, 0.f};
#pragma unroll
        for (int i = 0; i < 8; ++i) row_fma(acc, w[i], xr[i >> 1], i & 1);
        float u0, u1;
        reduce8(acc, u0, u1);
        bst(u0, u1, tag, ws, fsa, WS_F + kgrp * 14336);
        SB();
        ST(3);
        lds_wait<F_TRIG * 4>();
        __builtin_amdgcn_s_sleep(DELAY_F);
        u32x4 f0 = bld(ws, fa, WS_F), f1 = bld(ws, fa, WS_F + 4 * 14336);
        SB();
        lds_wait<20>();
        __builtin_amdgcn_s_sleep(DELAY_ML);
        u32x4 ml[8];
#pragma unroll
        for (int k = 0; k < 8; ++k) ml[k] = bld(ws, mla + k * 128, WS_ML + g * 4096 + (s >> 3) * 16);
        SB();
        ST(10);
        if (__builtin_expect(__builtin_amdgcn_ballot_w64(chk(chk(0, f0, tag), f1, tag) != 0) != 0, 0)) {
#pragma nounroll
            for (u32 n = 1; n < SPIN_CAP; ++n) {
                MEMBAR();
                f0 = bld(ws, fa, WS_F); f1 = bld(ws, fa, WS_F + 4 * 14336);
                CNT(8, n);
                if (__builtin_amdgcn_ballot_w64(chk(chk(0, f0, tag), f1, tag) != 0) == 0) break;
            }
        }
        ST(4);
        float h0 = __uint_as_float(f0[0]) + __uint_as_float(f1[0]), h1 = __uint_as_float(f0[2]) + __uint_as_float(f1[2]);
        h0 = sw16_add(h0, h0); h1 = sw16_add(h1, h1);
        h0 = sw32_add(h0, h0) + bb0; h1 = sw32_add(h1, h1) + bb1;
        constexpr float GC = -2.f * 0.7978845608028654f * 1.4426950408889634f;
        h0 = h0 * __builtin_amdgcn_rcpf(1.f + __builtin_amdgcn_exp2f(GC * fmaf(0.044715f * h0 * h0, h0, h0)));
        h1 = h1 * __builtin_amdgcn_rcpf(1.f + __builtin_amdgcn_exp2f(GC * fmaf(0.044715f * h1 * h1, h1, h1)));
        *(volatile LAS u32*)(lds + ha) = cvtpk(h0 * g1, h1 * g1);
        flag[7] = 1;
        SB();
        ST(5);
        {
            u32 bad = 0;
#pragma unroll
            for (int k = 0; k < 8; ++k) bad = chk(bad, ml[k], tag);
            if (__builtin_expect(__builtin_amdgcn_ballot_w64(bad != 0) != 0, 0)) {
#pragma nounroll
                for (u32 n = 1; n < SPIN_CAP; ++n) {
                    MEMBAR();
#pragma unroll
                    for (int k = 0; k < 8; ++k) ml[k] = bld(ws, mla + k * 128, WS_ML + g * 4096 + (s >> 3) * 16);
                    bad = 0;
#pragma unroll
                    for (int k = 0; k < 8; ++k) bad = chk(bad, ml[k], tag);
                    if (__builtin_amdgcn_ballot_w64(bad != 0) == 0) break;
                }
            }
        }
        float M = fmx(fmx(fmx(__uint_as_float(ml[0][0]), __uint_as_float(ml[1][0])), fmx(__uint_as_float(ml[2][0]), __uint_as_float(ml[3][0]))),
                      fmx(fmx(__uint_as_float(ml[4][0]), __uint_as_float(ml[5][0])), fmx(__uint_as_float(ml[6][0]), __uint_as_float(ml[7][0]))));
        M = sw16_max(M); M = sw32_max(M);
        float wg[8], L = 0.f;
#pragma unroll
        for (int k = 0; k < 8; ++k) { wg[k] = __builtin_amdgcn_exp2f(__uint_as_float(ml[k][0]) - M); L = fmaf(wg[k], __uint_as_float(ml[k][2]), L); }
        L = sw16_add(L, L); L = sw32_add(L, L);
        const float sc = (1.f - g1) * __builtin_amdgcn_rcpf(L);
        *(volatile LAS u32x4*)(lds + wla) = u32x4{cvtpk(wg[0] * sc, wg[1] * sc), cvtpk(wg[2] * sc, wg[3] * sc), cvtpk(wg[4] * sc, wg[5] * sc), cvtpk(wg[6] * sc, wg[7] * sc)};
        flag[9] = 1;
        SB();
        ST(1);
        u32x4 y;
        if (PRIME & 2) { y = bld(ws, ya, WS_Y + (j >> 6) * 65536 + (j & 63) * 16); PIN(y); }
        lds_wait<32>();
        __builtin_amdgcn_s_sleep(DELAY_Y);
#pragma nounroll
        for (u32 n = 0; n < SPIN_CAP; ++n) {
            MEMBAR();
            y = bld(ws, ya, WS_Y + (j >> 6) * 65536 + (j & 63) * 16);
            const u32 bad = chk(0, y, tag);
            CNT(9, n); STF(7, n);
            if (__builtin_amdgcn_ballot_w64(bad != 0) == 0) break;
        }
        ST(6);
        float r = sw32_add(__uint_as_float(y[0]), __uint_as_float(y[2]));
        r = sw16_add(r, r); r += dpp<ROR8>(r); r += dpp<ROR4>(r); r += dpp<XOR2>(r); r += dpp<XOR1>(r);
        r += bias;
        __builtin_amdgcn_raw_buffer_store_b16((unsigned short)cvtpk(r, r), mk(a.out + col * 2, 4), outa, 0, 0);
        __builtin_amdgcn_raw_buffer_store_b32(tag, ws, epa, WS_EPOCH, 16);
        ST_OUT();
    }
    asm volatile("" : "+s"(wv));
    if (wv == 3) {
        const int kg = lane >> 4;
        const rsrc_t r2 = mk(a.w2 + rho * (28 * 1024) + cq * 256, 28 * 1024 - cq * 256);
        const rsrc_t ro = mk(a.wo + rho * (64 * 1024) + cq * 256, 64 * 1024 - cq * 256);
        u32 w2a = kg * 8192 + (lane & 15) * 16, w2b = kg == 3 ? w2a - 4096 : w2a;
        u32 woa = (32 * (kg & 1) + 4 * (kg >> 1)) * 1024 + (lane & 15) * 16;
        u32 oga = kg * 2048 + (lane & 15) * 16, l16 = lane * 16, aa = L_A + kg * 16;
        PIN(w2a); PIN(w2b); PIN(woa); PIN(oga); PIN(l16); PIN(aa);
        if (PREW & 8) bst(0.f, 0.f, 0, ws, l16, WS_Y + cq * 65536 + rho * 1024);
        lds_wait<0>();
        u32x4 r[8], u[16], t2[8], ta[8], tb[8];
#pragma unroll
        for (int i = 0; i < 8; ++i) r[i] = bnt(r2, (i < 4 ? w2a : w2b) + (i & 3) * 1024, (i >> 2) * 4096);
        SB();
        ST(1);
        lds_wait<16>();
#pragma unroll
        for (int i = 0; i < 16; ++i) u[i] = bnt(ro, woa + (2 * (i >> 3) + ((i >> 2) & 1)) * 1024, (i & 3) * 8192);
        SB();
        ST(2);
        u32 tag = flag[10];
        PIN(tag); PIN(cw);
        transpose8(r, t2);
#pragma unroll
        for (int i = 0; i < 8; ++i) PIN(t2[i]);
        lds_wait<28>();
        const u32x4 af = *(volatile LAS u32x4*)(lds + aa);
        f32x4 d[8];
#pragma unroll
        for (int e = 0; e < 8; ++e) { d[e] = mfma(af, t2[e], f32x4{0.f, 0.f, 0.f, 0.f}); PIN(d[e]); }
        SB();
        ST(3);
        transpose8(u, ta);
        transpose8(u + 8, tb);
#pragma unroll
        for (int i = 0; i < 8; ++i) { PIN(ta[i]); PIN(tb[i]); }
        ST(5);
        lds_wait<24>();
        { const u64 t0 = __builtin_amdgcn_s_memtime(); lds_wait<36>(); while (__builtin_amdgcn_s_memtime() - t0 < (u64)(DELAY_O * 64)) {} }
        ST(4);
        u32x4 og[8];
#pragma unroll
        for (int k = 0; k < 8; ++k) og[k] = bld(ws, oga + k * 256, WS_O + rho * 8192);
        SB();
        u32x4 wf = *(volatile LAS u32x4*)(lds + aa + (L_W - L_A));
        PIN(wf);
        SB();
        {
            u32 bad = 0;
#pragma unroll
            for (int k = 0; k < 8; ++k) bad = chk(bad, og[k], tag);
            ST(10);
            if (__builtin_expect(__builtin_amdgcn_ballot_w64(bad != 0) != 0, 0)) {
#pragma nounroll
                for (u32 n = 1; n < SPIN_CAP; ++n) {
                    MEMBAR();
#pragma unroll
                    for (int k = 0; k < 8; ++k) og[k] = bld(ws, oga + k * 256, WS_O + rho * 8192);
                    bad = 0;
#pragma unroll
                    for (int k = 0; k < 8; ++k) bad = chk(bad, og[k], tag);
                    CNT(9, n);
                    if (__builtin_amdgcn_ballot_w64(bad != 0) == 0) break;
                }
            }
        }
        ST(7);
        f32x4 c[4];
#pragma unroll
        for (int e = 0; e < 4; ++e) {
            u32x4 ot;
#pragma unroll
            for (int p = 0; p < 4; ++p) ot[p] = __builtin_amdgcn_perm(og[2 * p + 1][e & 2], og[2 * p][e & 2], (e & 1) ? 0x07060302u : 0x05040100u);
            c[e] = mfma(ot, wf, f32x4{0.f, 0.f, 0.f, 0.f});
        }
        const u32x4 a0 = {cvtpk(c[0][0], c[0][1]), cvtpk(c[0][2], c[0][3]), cvtpk(c[1][0], c[1][1]), cvtpk(c[1][2], c[1][3])};
        const u32x4 a1 = {cvtpk(c[2][0], c[2][1]), cvtpk(c[2][2], c[2][3]), cvtpk(c[3][0], c[3][1]), cvtpk(c[3][2], c[3][3])};
#pragma unroll
        for (int e = 0; e < 8; ++e) d[e] = mfma(a0, ta[e], d[e]);
#pragma unroll
        for (int e = 0; e < 8; ++e) d[e] = mfma(a1, tb[e], d[e]);
        const bool k0 = kg & 1, k1 = kg & 2;
        const float ya0 = k0 ? d[2][0] : d[0][0], yb0 = k0 ? d[6][0] : d[4][0], ya1 = k0 ? d[3][0] : d[1][0], yb1 = k0 ? d[7][0] : d[5][0];
        const float y0 = k1 ? yb0 : ya0, y1 = k1 ? yb1 : ya1;
        bst(y0, y1, tag, ws, l16, WS_Y + cq * 65536 + rho * 1024);
        flag[8] = 1;
        ST_OUT();
    }
}

extern "C" void kernel_launch(void* const* d_in, const int* in_sizes, int n_in, void* d_out, int out_size, void* d_ws, size_t ws_size, hipStream_t stream) {
    Args a;
    a.x = (const char*)d_in[0]; a.k = (const char*)d_in[1]; a.v = (const char*)d_in[2]; a.wq = (const char*)d_in[3]; a.wo = (const char*)d_in[4];
    a.w1 = (const char*)d_in[6]; a.b1 = (const char*)d_in[7]; a.w2 = (const char*)d_in[8]; a.b2 = (const char*)d_in[9]; a.gw = (const char*)d_in[10];
    a.out = (char*)d_out; a.ws = (char*)d_ws + (WSOFF ? ws_size - (8u << 20) : 0);
    gated_mix_kernel<<<256, 256, 0, stream>>>(a);
}
```

```cpp
#define PROBE 0
#include <hip/hip_runtime.h>
#include <stdint.h>

typedef unsigned u32;
typedef unsigned long long u64;
typedef u32 u32x4 __attribute__((ext_vector_type(4)));
typedef float f32x4 __attribute__((ext_vector_type(4)));
typedef __bf16 bf16x8 __attribute__((ext_vector_type(8)));
typedef __bf16 bf16x2 __attribute__((ext_vector_type(2)));
#define GAS __attribute__((address_space(1)))
#define LAS __attribute__((address_space(3)))
#define SB() __builtin_amdgcn_sched_barrier(0)
#define MEMBAR() asm volatile("" ::: "memory")

#define GATHER_AT 9
#define VHOLD 0
#define DELAY_ML 8
#define DELAY_O 8
#define DELAY_Y 8
#define WO_FIRST 0
#define DELAY_F 8
#define F_TRIG 4
#define K0 32
#define REL_F 25
#define REL_T 9
#define VWIN 32
#define PRIME 0
#define PREW 0
#define WSOFF 0

constexpr int SK = 65536, WIN = 4096;
constexpr u32 SPIN_CAP = 1u << 16;
constexpr u32 WS_EPOCH = 0;
constexpr u32 WS_Q = 4096;
constexpr u32 WS_ML = WS_Q + 8 * 16384;
constexpr u32 WS_F = WS_ML + 32768;
constexpr u32 WS_O = WS_F + 8 * 896 * 16;
constexpr u32 WS_Y = WS_O + (1u << 19);
constexpr u32 WS_END = WS_Y + (1u << 18);
constexpr u32 WS_STAMP = 4u << 20;
constexpr int L_FLAG = 0;
constexpr int L_DUMMY = 48;
constexpr int L_A = 64;
constexpr int L_W = 384;
constexpr int L_Z = 128;
constexpr int L_Q = 512;
constexpr int L_P = L_Q + 1280;
constexpr int L_END = L_P + 1280;

struct Args {
    const char* x; const char* k; const char* v; const char* wq; const char* wo;
    const char* w1; const char* b1; const char* w2; const char* b2; const char* gw;
    char* out; char* ws;
};

__device__ __forceinline__ u64 rt() { u64 t; SB(); asm volatile("s_memrealtime %0\n\ts_waitcnt lgkmcnt(0)" : "=s"(t) :: "memory"); SB(); return t; }
#define ST(i) do { if (ROLE * 16 + (i) == PROBE) flag[11] = (u32)(rt() - t_start) + 1; } while (0)
#define CNT(i, n)
#define STF(i, n) do { if (ROLE * 16 + (i) == PROBE && (n) == 0) flag[11] = (u32)(rt() - t_start) + 1; } while (0)
#define ST_DECL const u64 t_start = rt()
#define ST_OUT() ST(11)
#define PROBE_G -1

__device__ __forceinline__ u32x4 ldnt(const char* p) { return __builtin_nontemporal_load((const GAS u32x4*)p); }
typedef __amdgpu_buffer_rsrc_t rsrc_t;
__device__ __forceinline__ rsrc_t mk(const char* p, u32 n) { return __builtin_amdgcn_make_buffer_rsrc((void*)p, 0, n, 0x00020000); }
__device__ __forceinline__ u32x4 bnt(rsrc_t r, u32 v, u32 s) { return __builtin_amdgcn_raw_buffer_load_b128(r, v, s, 2); }
__device__ __forceinline__ u32x4 bld(rsrc_t r, u32 v, u32 s) { return __builtin_amdgcn_raw_buffer_load_b128(r, v, s, 16); }
__device__ __forceinline__ void bstu(u32 a, u32 b, u32 tag, rsrc_t r, u32 v, u32 s) { __builtin_amdgcn_raw_buffer_store_b128(u32x4{a, tag, b, tag}, r, v, s, 16); }
__device__ __forceinline__ u32 chk(u32 bad, u32x4 x, u32 tag) { bad |= x[1] ^ tag; asm volatile("" : "+v"(bad)); bad |= x[3] ^ tag; asm volatile("" : "+v"(bad)); return bad; }
__device__ __forceinline__ void bst(float a, float b, u32 tag, rsrc_t r, u32 v, u32 s) { __builtin_amdgcn_raw_buffer_store_b128(u32x4{__float_as_uint(a), tag, __float_as_uint(b), tag}, r, v, s, 16); }
#define PIN(x) asm volatile("" : "+v"(x))
constexpr u32 OOB = 0x80000000u;
constexpr u32 CODE_BYTES = 12800;
__device__ __forceinline__ float dot2(u32 a, u32 b, float c) { return __builtin_amdgcn_fdot2_f32_bf16(__builtin_bit_cast(bf16x2, a), __builtin_bit_cast(bf16x2, b), c, false); }
__device__ __forceinline__ u32 cvtpk(float lo, float hi) { bf16x2 r = {(__bf16)lo, (__bf16)hi}; return __builtin_bit_cast(u32, r); }
template <int CTRL> __device__ __forceinline__ float dpp(float x) { return __builtin_bit_cast(float, __builtin_amdgcn_mov_dpp(__builtin_bit_cast(int, x), CTRL, 0xf, 0xf, true)); }
constexpr int XOR1 = 0xB1, XOR2 = 0x4E, ROR4 = 0x124, ROR8 = 0x128;
__device__ __forceinline__ float sw16_add(float a, float b) { auto s = __builtin_amdgcn_permlane16_swap(__float_as_uint(a), __float_as_uint(b), false, false); return __uint_as_float(s[0]) + __uint_as_float(s[1]); }
__device__ __forceinline__ float sw32_add(float a, float b) { auto s = __builtin_amdgcn_permlane32_swap(__float_as_uint(a), __float_as_uint(b), false, false); return __uint_as_float(s[0]) + __uint_as_float(s[1]); }
__device__ __forceinline__ float fmx(float a, float b) { return __builtin_elementwise_maximum(a, b); }
__device__ __forceinline__ float sw16_max(float a) { auto s = __builtin_amdgcn_permlane16_swap(__float_as_uint(a), __float_as_uint(a), false, false); return fmx(__uint_as_float(s[0]), __uint_as_float(s[1])); }
__device__ __forceinline__ float sw32_max(float a) { auto s = __builtin_amdgcn_permlane32_swap(__float_as_uint(a), __float_as_uint(a), false, false); return fmx(__uint_as_float(s[0]), __uint_as_float(s[1])); }
__device__ __forceinline__ f32x4 mfma(u32x4 a, u32x4 b, f32x4 c) { return __builtin_amdgcn_mfma_f32_16x16x32_bf16(__builtin_bit_cast(bf16x8, a), __builtin_bit_cast(bf16x8, b), c, 0, 0, 0); }
__device__ __forceinline__ float bf_lo(u32 w) { return __uint_as_float(w << 16); }
__device__ __forceinline__ float bf_hi(u32 w) { return __uint_as_float(w & 0xffff0000u); }

__device__ __forceinline__ void transpose8(const u32x4* r, u32x4* t) {
#pragma unroll
    for (int p = 0; p < 4; ++p)
#pragma unroll
        for (int d = 0; d < 4; ++d) {
            t[2 * d][p] = __builtin_amdgcn_perm(r[2 * p + 1][d], r[2 * p][d], 0x05040100u);
            t[2 * d + 1][p] = __builtin_amdgcn_perm(r[2 * p + 1][d], r[2 * p][d], 0x07060302u);
        }
}
__device__ __forceinline__ void row_fma(float* acc, u32x4 w, u32 xpair, int odd) {
    const u32 xl = odd ? xpair >> 16 : xpair & 0xffffu, xh = odd ? xpair & 0xffff0000u : xpair << 16;
#pragma unroll
    for (int d = 0; d < 4; ++d) { acc[2 * d] = dot2(w[d], xl, acc[2 * d]); acc[2 * d + 1] = dot2(w[d], xh, acc[2 * d + 1]); }
}
__device__ __forceinline__ void reduce8(const float* acc, float& u0, float& u1) {
    const float s0 = sw32_add(acc[0], acc[4]), s1 = sw32_add(acc[1], acc[5]), s2 = sw32_add(acc[2], acc[6]), s3 = sw32_add(acc[3], acc[7]);
    u0 = sw16_add(s0, s2); u1 = sw16_add(s1, s3);
    u0 += dpp<ROR8>(u0); u1 += dpp<ROR8>(u1);
}
__device__ __forceinline__ float gate_of(u32x4 x, u32x4 gw) {
    float s = 0.f;
#pragma unroll
    for (int d = 0; d < 4; ++d) s = dot2(x[d], gw[d], s);
    s += dpp<XOR1>(s); s += dpp<XOR2>(s); s += dpp<ROR4>(s); s += dpp<ROR8>(s);
    s = sw16_add(s, s); s = sw32_add(s, s);
    return __builtin_amdgcn_rcpf(1.f + __builtin_amdgcn_exp2f(-1.4426950408889634f * s));
}
template <int OFF> __device__ __forceinline__ void lds_wait() {
    u32 t, n, c;
    asm volatile("s_mov_b32 %1, 0x100000\n\tv_mov_b32 %0, 0\n"
                 "1:\n\tds_read_b32 %0, %0 offset:%3\n\ts_waitcnt lgkmcnt(0)\n\tv_readfirstlane_b32 %2, %0\n\ts_cmp_lg_u32 %2, 0\n\ts_cbranch_scc1 2f\n\t"
                 "s_sleep 1\n\ts_sub_u32 %1, %1, 1\n\ts_cbranch_scc0 1b\n"
                 "2:" : "=&v"(t), "=&s"(n), "=&s"(c) : "n"(OFF) : "memory", "scc");
}
#define BARRIER() do { asm volatile("s_waitcnt lgkmcnt(0)" ::: "memory"); __builtin_amdgcn_s_barrier(); MEMBAR(); } while (0)

__global__ void __launch_bounds__(256, 2) gated_mix_kernel(Args a) {
    __shared__ __attribute__((aligned(16))) char lds_[L_END];
    LAS char* lds = (LAS char*)lds_;
    volatile LAS u32* flag = (volatile LAS u32*)(lds + L_FLAG);
    const int b = blockIdx.x, g = b & 7, s = b >> 3, lane = threadIdx.x & 63;
    int wv = __builtin_amdgcn_readfirstlane(threadIdx.x >> 6);
    ST_DECL;
    u64 pc; asm volatile("s_getpc_b64 %0" : "=s"(pc));
    if (threadIdx.x < L_Q / 4) *(volatile LAS u32*)(lds + threadIdx.x * 4) = 0;
    asm volatile("" :: "s"(a.x), "s"(a.k), "s"(a.v), "s"(a.wq), "s"(a.wo), "s"(a.w1), "s"(a.b1), "s"(a.w2), "s"(a.b2), "s"(a.gw), "s"(a.out), "s"(a.ws));
    BARRIER();
    const rsrc_t ws = mk(a.ws, WS_END);
    const u32 kvoff = (u32)(g * SK + (SK - WIN) + s * 128) * 256u;
    u32 cw = __builtin_amdgcn_raw_buffer_load_b32(mk((const char*)(pc & ~255ull), wv == 3 ? CODE_BYTES : 0), (lane & 15) * 128 + (lane >> 4) * 8192, (s & 3) * 2048, 0);
    SB();
    const int rho = 8 * g + (s >> 2), cq = s & 3;

    if (wv == 0) {
        constexpr int ROLE = 0; (void)ROLE;
        const int kq = s & 3, r8 = lane >> 3;
        ST(0);
        u32 tag = __builtin_amdgcn_raw_buffer_load_b32(ws, 0, WS_EPOCH, 16);
        u32x4 xr[2], w[16], kf[32];
        const char* xp = a.x + (kq * 128 + r8 * 16) * 2;
        xr[0] = *(const GAS u32x4*)xp; xr[1] = *(const GAS u32x4*)(xp + 16);
        const rsrc_t rq = mk(a.wq + kq * (128 * 8192) + g * 1024 + (s >> 2) * 128, 128 * 8192);
        const u32 wo_ = r8 * (16 * 8192) + (lane & 7) * 16;
#pragma unroll
        for (int i = 0; i < 16; ++i) { w[i] = bnt(rq, wo_, i * 8192); SB(); }
        const int key3 = (lane >> 1) & 7;
        const u32 ko = key3 * 256 + (2 * (lane >> 4) + (lane & 1)) * 16;
        const rsrc_t rk = mk(a.k + kvoff, 32768);
#pragma unroll
        for (int i = 0; i < K0; ++i) kf[i] = bnt(rk, ko + ((i >> 1) & 1) * 2048 + (i & 1) * 128, (i >> 2) * 4096);
        SB();
        tag += 1;
        flag[10] = tag;
        ST(1);
        const int hd = (lane >> 2) & 3, xb = (lane >> 1) & 1, yb = lane & 1;
        u32 qsa = (lane & 8) ? OOB : ((s >> 2) * 32 + 4 * (lane & 7) + 2 * (lane >> 5) + ((lane >> 4) & 1)) * 16;
        u32 qa0 = L_Q + hd * 320 + (2 * (lane >> 4) + yb) * 16, qa1 = xb ? qa0 : L_Z;
        qa0 = xb ? L_Z : qa0;
        u32 pwa = L_P + hd * 320 + (2 * xb + (lane >> 5)) * 64 + (2 * ((lane >> 4) & 1) + yb) * 16;
        u32 mla = (lane & 0x23) ? OOB : hd * 16 + (lane & 16) * 4;
        u32 l16 = lane * 16;
        PIN(qsa); PIN(qa0); PIN(qa1); PIN(pwa); PIN(mla); PIN(l16);
        SB();
        float acc[8] = {0.f, 0.f, 0.f, 0.f, 0.f, 0.f, 0.f, 0.f};
#pragma unroll
        for (int i = 0; i < 16; ++i) row_fma(acc, w[i], xr[i >> 3][(i >> 1) & 3], i & 1);
        float u0, u1;
        reduce8(acc, u0, u1);
        ST(2);
        bst(u0, u1, tag, ws, qsa, WS_Q + g * 16384 + kq * 4096);
        SB();
#pragma unroll
        for (int i = K0; i < 32; ++i) {
            if ((i & 3) == 0) { asm volatile("s_waitcnt vmcnt(%0)" :: "n"(K0 - 3) : "memory"); SB(); }
            kf[i] = bnt(rk, ko + ((i >> 1) & 1) * 2048 + (i & 1) * 128, (i >> 2) * 4096);
            SB();
        }
        asm volatile("s_waitcnt vmcnt(%0)" :: "n"(REL_F) : "memory");
        flag[0] = 1;
        asm volatile("s_waitcnt vmcnt(%0)" :: "n"(GATHER_AT) : "memory");
        ST(3);
        u32x4 qg[16];
#pragma nounroll
        for (u32 n = 0; n < SPIN_CAP; ++n) {
            MEMBAR();
#pragma unroll
            for (int i = 0; i < 16; ++i) qg[i] = bld(ws, l16 + (i >> 2) * 1024, WS_Q + g * 16384 + (i & 3) * 4096);
            SB();
            flag[3] = 1;
            SB();
            u32 bad = 0;
#pragma unroll
            for (int i = 0; i < 16; ++i) bad = chk(bad, qg[i], tag);
            CNT(8, n); STF(7, n);
            if (__builtin_amdgcn_ballot_w64(bad != 0) == 0) break;
        }
        ST(4);
#pragma unroll
        for (int t = 0; t < 4; ++t) {
            const float q0 = (__uint_as_float(qg[4 * t][0]) + __uint_as_float(qg[4 * t + 1][0])) + (__uint_as_float(qg[4 * t + 2][0]) + __uint_as_float(qg[4 * t + 3][0]));
            const float q1 = (__uint_as_float(qg[4 * t][2]) + __uint_as_float(qg[4 * t + 1][2])) + (__uint_as_float(qg[4 * t + 2][2]) + __uint_as_float(qg[4 * t + 3][2]));
            *(volatile LAS u32*)(lds + L_Q + t * 320 + lane * 4) = cvtpk(q0, q1);
        }
        u32x4 qf[2][2];
        qf[0][0] = *(volatile LAS u32x4*)(lds + qa0); qf[0][1] = *(volatile LAS u32x4*)(lds + qa0 + 128);
        qf[1][0] = *(volatile LAS u32x4*)(lds + qa1); qf[1][1] = *(volatile LAS u32x4*)(lds + qa1 + 128);
        SB();
        float sc[8];
#pragma unroll
        for (int n = 0; n < 8; ++n) {
            f32x4 d = {0.f, 0.f, 0.f, 0.f};
            d = mfma(kf[4 * n], qf[0][0], d); d = mfma(kf[4 * n + 1], qf[0][1], d);
            d = mfma(kf[4 * n + 2], qf[1][0], d); d = mfma(kf[4 * n + 3], qf[1][1], d);
            const float p = yb ? d[3] : d[0], q = yb ? d[1] : d[2];
            sc[n] = p + dpp<XOR1>(q);
        }
        ST(5);
        constexpr float C2 = 0.08838834764831845f * 1.4426950408889634f;
        float m = fmx(fmx(fmx(sc[0], sc[1]), fmx(sc[2], sc[3])), fmx(fmx(sc[4], sc[5]), fmx(sc[6], sc[7])));
        m = fmx(m, dpp<XOR1>(m)); m = fmx(m, dpp<XOR2>(m)); m = sw16_max(m); m = sw32_max(m);
        m *= C2;
        float p[8];
#pragma unroll
        for (int n = 0; n < 8; ++n) p[n] = __builtin_amdgcn_exp2f(fmaf(sc[n], C2, -m));
        *(volatile LAS u32x4*)(lds + pwa) = u32x4{cvtpk(p[0], p[1]), cvtpk(p[2], p[3]), cvtpk(p[4], p[5]), cvtpk(p[6], p[7])};
        flag[5] = 1;
        SB();
        ST(6);
        float l = ((p[0] + p[1]) + (p[2] + p[3])) + ((p[4] + p[5]) + (p[6] + p[7]));
        l += dpp<XOR1>(l); l += dpp<XOR2>(l); l = sw16_add(l, l); l = sw32_add(l, l);
        bst(m, l, tag, ws, mla, WS_ML + g * 4096 + s * 128);
        ST_OUT();
    }
    asm volatile("" : "+s"(wv));
    if (wv == 1) {
        constexpr int ROLE = 1; (void)ROLE;
        u32 oa = ((lane >> 4) * 2 + ((lane >> 3) & 1)) * 8192 + (lane & 7) * 16;
        u32 pa = L_P + ((lane >> 2) & 3) * 320 + (lane >> 4) * 16;
        u32 l16 = lane * 16;
        PIN(oa); PIN(pa); PIN(l16);
        const rsrc_t rv = mk(a.v + kvoff, 32768);
        if (PREW & 2) { bst(0.f, 0.f, 0, ws, oa, WS_O + g * 65536 + s * 256); bst(0.f, 0.f, 0, ws, oa + 128, WS_O + g * 65536 + s * 256); }
        lds_wait<12>();
        ST(1);
        u32x4 v[32];
#pragma unroll
        for (int i = 0; i < 32; ++i) {
            if (i >= VWIN && (i & 3) == 0) { SB(); asm volatile("s_waitcnt vmcnt(%0)" :: "n"(VWIN - 4) : "memory"); SB(); }
            v[i] = bnt(rv, l16 + (i >> 3) * 1024, (i & 7) * 4096);
        }
        SB();
        flag[4] = 1;
        u32 tag = flag[10]; PIN(tag);
        ST(2);
        u32x4 t0[8], t1[8], t2[8], t3[8], pf[4];
        transpose8(v, t0);
        transpose8(v + 8, t1);
        transpose8(v + 16, t2);
#pragma unroll
        for (int i = 0; i < 8; ++i) { PIN(t0[i]); PIN(t1[i]); PIN(t2[i]); }
        SB();
#pragma nounroll
        for (u32 n = 0; n < SPIN_CAP * 16; ++n) {
            const u32 f = flag[5];
#pragma unroll
            for (int i = 0; i < 4; ++i) pf[i] = *(volatile LAS u32x4*)(lds + pa + i * 64);
            if (__builtin_amdgcn_readfirstlane(f)) break;
        }
        ST(3);
        f32x4 o[8];
#pragma unroll
        for (int e = 0; e < 8; ++e) { o[e] = mfma(pf[0], t0[e], f32x4{0.f, 0.f, 0.f, 0.f}); }
#pragma unroll
        for (int e = 0; e < 8; ++e) o[e] = mfma(pf[1], t1[e], o[e]);
#pragma unroll
        for (int e = 0; e < 8; ++e) o[e] = mfma(pf[2], t2[e], o[e]);
        transpose8(v + 24, t3);
        ST(4);
#pragma unroll
        for (int e = 0; e < 8; ++e) o[e] = mfma(pf[3], t3[e], o[e]);
#pragma unroll
        for (int k = 0; k < 2; ++k) bstu(cvtpk(o[4 * k][0], o[4 * k + 1][0]), cvtpk(o[4 * k + 2][0], o[4 * k + 3][0]), tag, ws, oa + k * 128, WS_O + g * 65536 + s * 256);
        flag[6] = 1;
        ST_OUT();
    }
    asm volatile("" : "+s"(wv));
    if (wv == 2) {
        constexpr int ROLE = 2; (void)ROLE;
        const int bb = b < 224 ? b : b - 224, kgrp = bb / 28, cgrp = bb - kgrp * 28, r8 = lane >> 3;
        const int j = g * 32 + s, col = 128 * (j >> 6) + 8 * (j & 15) + 2 * ((j >> 4) & 3);
        const u32x4 xa = *(const GAS u32x4*)(a.x + lane * 16), gw = *(const GAS u32x4*)(a.gw + lane * 16);
        const u32x4 xr = *(const GAS u32x4*)(a.x + (kgrp * 64 + r8 * 8) * 2);
        const int pr = (lane & 15) < 14 ? (lane & 15) : 13;
        const u32 b1w = *(const GAS u32*)(a.b1 + (rho * 14 + pr) * 4);
        const u32 b2w = __builtin_amdgcn_raw_buffer_load_b32(mk(a.b2, 1024), 0, col * 2, 0);
        SB();
        const rsrc_t r1 = mk(a.w1 + kgrp * (64 * 3584) + cgrp * 128, b < 224 ? 64 * 3584 - cgrp * 128 : 0);
        u32 wa = r8 * (8 * 3584) + (lane & 7) * 16;
        u32 fsa = ((lane & 8) || b >= 224) ? OOB : (cgrp * 32 + 4 * (lane & 7) + 2 * (lane >> 5) + ((lane >> 4) & 1)) * 16;
        u32 fa = (lane >> 4) * 14336 + (rho * 14 + pr) * 16;
        u32 ha = lane < 14 ? L_A + lane * 4 : L_DUMMY;
        u32 ya = lane * 1024, outa = (lane & 31) ? OOB : (lane >> 4), epa = (b | lane) ? OOB : 0;
        u32 mla = (lane >> 4) * 1024, wla = L_W + (lane >> 4) * 16;
        PIN(mla); PIN(wla);
        PIN(wa); PIN(fsa); PIN(fa); PIN(ha); PIN(ya); PIN(outa); PIN(epa);
        lds_wait<0>();
        u32x4 w[8];
#pragma unroll
        for (int i = 0; i < 8; ++i) w[i] = bnt(r1, wa, i * 3584);
        SB();
        ST(2);
        float g1 = 1.f - gate_of(xa, gw), bb0 = bf_lo(b1w), bb1 = bf_hi(b1w);
        float bias = g1 * (lane < 32 ? bf_lo(b2w) : bf_hi(b2w));
        u32 tag = flag[10];
        PIN(g1); PIN(bb0); PIN(bb1); PIN(bias); PIN(tag);
        SB();
        float acc[8] = {0.f, 0.f, 0.f, 0.f, 0.f, 0.f, 0.f, 0.f};
#pragma unroll
        for (int i = 0; i < 8; ++i) row_fma(acc, w[i], xr[i >> 1], i & 1);
        float u0, u1;
        reduce8(acc, u0, u1);
        bst(u0, u1, tag, ws, fsa, WS_F + kgrp * 14336);
        SB();
        ST(3);
        lds_wait<F_TRIG * 4>();
        __builtin_amdgcn_s_sleep(DELAY_F);
        u32x4 f0 = bld(ws, fa, WS_F), f1 = bld(ws, fa, WS_F + 4 * 14336);
        SB();
        lds_wait<20>();
        __builtin_amdgcn_s_sleep(DELAY_ML);
        u32x4 ml[8];
#pragma unroll
        for (int k = 0; k < 8; ++k) ml[k] = bld(ws, mla + k * 128, WS_ML + g * 4096 + (s >> 3) * 16);
        SB();
        ST(10);
        if (__builtin_expect(__builtin_amdgcn_ballot_w64(chk(chk(0, f0, tag), f1, tag) != 0) != 0, 0)) {
#pragma nounroll
            for (u32 n = 1; n < SPIN_CAP; ++n) {
                MEMBAR();
                f0 = bld(ws, fa, WS_F); f1 = bld(ws, fa, WS_F + 4 * 14336);
                CNT(8, n);
                if (__builtin_amdgcn_ballot_w64(chk(chk(0, f0, tag), f1, tag) != 0) == 0) break;
            }
        }
        ST(4);
        float h0 = __uint_as_float(f0[0]) + __uint_as_float(f1[0]), h1 = __uint_as_float(f0[2]) + __uint_as_float(f1[2]);
        h0 = sw16_add(h0, h0); h1 = sw16_add(h1, h1);
        h0 = sw32_add(h0, h0) + bb0; h1 = sw32_add(h1, h1) + bb1;
        constexpr float GC = -2.f * 0.7978845608028654f * 1.4426950408889634f;
        h0 = h0 * __builtin_amdgcn_rcpf(1.f + __builtin_amdgcn_exp2f(GC * fmaf(0.044715f * h0 * h0, h0, h0)));
        h1 = h1 * __builtin_amdgcn_rcpf(1.f + __builtin_amdgcn_exp2f(GC * fmaf(0.044715f * h1 * h1, h1, h1)));
        *(volatile LAS u32*)(lds + ha) = cvtpk(h0 * g1, h1 * g1);
        flag[7] = 1;
        SB();
        ST(5);
        {
            u32 bad = 0;
#pragma unroll
            for (int k = 0; k < 8; ++k) bad = chk(bad, ml[k], tag);
            if (__builtin_expect(__builtin_amdgcn_ballot_w64(bad != 0) != 0, 0)) {
#pragma nounroll
                for (u32 n = 1; n < SPIN_CAP; ++n) {
                    MEMBAR();
#pragma unroll
                    for (int k = 0; k < 8; ++k) ml[k] = bld(ws, mla + k * 128, WS_ML + g * 4096 + (s >> 3) * 16);
                    bad = 0;
#pragma unroll
                    for (int k = 0; k < 8; ++k) bad = chk(bad, ml[k], tag);
                    if (__builtin_amdgcn_ballot_w64(bad != 0) == 0) break;
                }
            }
        }
        float M = fmx(fmx(fmx(__uint_as_float(ml[0][0]), __uint_as_float(ml[1][0])), fmx(__uint_as_float(ml[2][0]), __uint_as_float(ml[3][0]))),
                      fmx(fmx(__uint_as_float(ml[4][0]), __uint_as_float(ml[5][0])), fmx(__uint_as_float(ml[6][0]), __uint_as_float(ml[7][0]))));
        M = sw16_max(M); M = sw32_max(M);
        float wg[8], L = 0.f;
#pragma unroll
        for (int k = 0; k < 8; ++k) { wg[k] = __builtin_amdgcn_exp2f(__uint_as_float(ml[k][0]) - M); L = fmaf(wg[k], __uint_as_float(ml[k][2]), L); }
        L = sw16_add(L, L); L = sw32_add(L, L);
        const float sc = (1.f - g1) * __builtin_amdgcn_rcpf(L);
        *(volatile LAS u32x4*)(lds + wla) = u32x4{cvtpk(wg[0] * sc, wg[1] * sc), cvtpk(wg[2] * sc, wg[3] * sc), cvtpk(wg[4] * sc, wg[5] * sc), cvtpk(wg[6] * sc, wg[7] * sc)};
        flag[9] = 1;
        SB();
        ST(1);
        u32x4 y;
        if (PRIME & 2) { y = bld(ws, ya, WS_Y + (j >> 6) * 65536 + (j & 63) * 16); PIN(y); }
        lds_wait<32>();
        __builtin_amdgcn_s_sleep(DELAY_Y);
#pragma nounroll
        for (u32 n = 0; n < SPIN_CAP; ++n) {
            MEMBAR();
            y = bld(ws, ya, WS_Y + (j >> 6) * 65536 + (j & 63) * 16);
            const u32 bad = chk(0, y, tag);
            CNT(9, n); STF(7, n);
            if (__builtin_amdgcn_ballot_w64(bad != 0) == 0) break;
        }
        ST(6);
        float r = sw32_add(__uint_as_float(y[0]), __uint_as_float(y[2]));
        r = sw16_add(r, r); r += dpp<ROR8>(r); r += dpp<ROR4>(r); r += dpp<XOR2>(r); r += dpp<XOR1>(r);
        r += bias;
        __builtin_amdgcn_raw_buffer_store_b16((unsigned short)cvtpk(r, r), mk(a.out + col * 2, 4), outa, 0, 0);
        __builtin_amdgcn_raw_buffer_store_b32(tag, ws, epa, WS_EPOCH, 16);
        ST_OUT();
        { lds_wait<44>(); const u32 dv = __builtin_amdgcn_readfirstlane(flag[11]) - 1; const u64 te = rt() + 10ull * dv; if (PROBE_G < 0 || g == PROBE_G) while (rt() < te) __builtin_amdgcn_s_sleep(1); }
    }
    asm volatile("" : "+s"(wv));
    if (wv == 3) {
        constexpr int ROLE = 3; (void)ROLE;
        const int kg = lane >> 4;
        const rsrc_t r2 = mk(a.w2 + rho * (28 * 1024) + cq * 256, 28 * 1024 - cq * 256);
        const rsrc_t ro = mk(a.wo + rho * (64 * 1024) + cq * 256, 64 * 1024 - cq * 256);
        u32 w2a = kg * 8192 + (lane & 15) * 16, w2b = kg == 3 ? w2a - 4096 : w2a;
        u32 woa = (32 * (kg & 1) + 4 * (kg >> 1)) * 1024 + (lane & 15) * 16;
        u32 oga = kg * 2048 + (lane & 15) * 16, l16 = lane * 16, aa = L_A + kg * 16;
        PIN(w2a); PIN(w2b); PIN(woa); PIN(oga); PIN(l16); PIN(aa);
        if (PREW & 8) bst(0.f, 0.f, 0, ws, l16, WS_Y + cq * 65536 + rho * 1024);
        lds_wait<0>();
        u32x4 r[8], u[16], t2[8], ta[8], tb[8];
#pragma unroll
        for (int i = 0; i < 8; ++i) r[i] = bnt(r2, (i < 4 ? w2a : w2b) + (i & 3) * 1024, (i >> 2) * 4096);
        SB();
        ST(1);
        lds_wait<16>();
#pragma unroll
        for (int i = 0; i < 16; ++i) u[i] = bnt(ro, woa + (2 * (i >> 3) + ((i >> 2) & 1)) * 1024, (i & 3) * 8192);
        SB();
        ST(2);
        u32 tag = flag[10];
        PIN(tag); PIN(cw);
        transpose8(r, t2);
#pragma unroll
        for (int i = 0; i < 8; ++i) PIN(t2[i]);
        lds_wait<28>();
        const u32x4 af = *(volatile LAS u32x4*)(lds + aa);
        f32x4 d[8];
#pragma unroll
        for (int e = 0; e < 8; ++e) { d[e] = mfma(af, t2[e], f32x4{0.f, 0.f, 0.f, 0.f}); PIN(d[e]); }
        SB();
        ST(3);
        transpose8(u, ta);
        transpose8(u + 8, tb);
#pragma unroll
        for (int i = 0; i < 8; ++i) { PIN(ta[i]); PIN(tb[i]); }
        ST(5);
        lds_wait<24>();
        { const u64 t0 = __builtin_amdgcn_s_memtime(); lds_wait<36>(); while (__builtin_amdgcn_s_memtime() - t0 < (u64)(DELAY_O * 64)) {} }
        ST(4);
        u32x4 og[8];
#pragma unroll
        for (int k = 0; k < 8; ++k) og[k] = bld(ws, oga + k * 256, WS_O + rho * 8192);
        SB();
        u32x4 wf = *(volatile LAS u32x4*)(lds + aa + (L_W - L_A));
        PIN(wf);
        SB();
        {
            u32 bad = 0;
#pragma unroll
            for (int k = 0; k < 8; ++k) bad = chk(bad, og[k], tag);
            ST(10);
            if (__builtin_expect(__builtin_amdgcn_ballot_w64(bad != 0) != 0, 0)) {
#pragma nounroll
                for (u32 n = 1; n < SPIN_CAP; ++n) {
                    MEMBAR();
#pragma unroll
                    for (int k = 0; k < 8; ++k) og[k] = bld(ws, oga + k * 256, WS_O + rho * 8192);
                    bad = 0;
#pragma unroll
                    for (int k = 0; k < 8; ++k) bad = chk(bad, og[k], tag);
                    CNT(9, n);
                    if (__builtin_amdgcn_ballot_w64(bad != 0) == 0) break;
                }
            }
        }
        ST(7);
        f32x4 c[4];
#pragma unroll
        for (int e = 0; e < 4; ++e) {
            u32x4 ot;
#pragma unroll
            for (int p = 0; p < 4; ++p) ot[p] = __builtin_amdgcn_perm(og[2 * p + 1][e & 2], og[2 * p][e & 2], (e & 1) ? 0x07060302u : 0x05040100u);
            c[e] = mfma(ot, wf, f32x4{0.f, 0.f, 0.f, 0.f});
        }
        const u32x4 a0 = {cvtpk(c[0][0], c[0][1]), cvtpk(c[0][2], c[0][3]), cvtpk(c[1][0], c[1][1]), cvtpk(c[1][2], c[1][3])};
        const u32x4 a1 = {cvtpk(c[2][0], c[2][1]), cvtpk(c[2][2], c[2][3]), cvtpk(c[3][0], c[3][1]), cvtpk(c[3][2], c[3][3])};
#pragma unroll
        for (int e = 0; e < 8; ++e) d[e] = mfma(a0, ta[e], d[e]);
#pragma unroll
        for (int e = 0; e < 8; ++e) d[e] = mfma(a1, tb[e], d[e]);
        const bool k0 = kg & 1, k1 = kg & 2;
        const float ya0 = k0 ? d[2][0] : d[0][0], yb0 = k0 ? d[6][0] : d[4][0], ya1 = k0 ? d[3][0] : d[1][0], yb1 = k0 ? d[7][0] : d[5][0];
        const float y0 = k1 ? yb0 : ya0, y1 = k1 ? yb1 : ya1;
        bst(y0, y1, tag, ws, l16, WS_Y + cq * 65536 + rho * 1024);
        flag[8] = 1;
        ST_OUT();
    }
}

extern "C" void kernel_launch(void* const* d_in, const int* in_sizes, int n_in, void* d_out, int out_size, void* d_ws, size_t ws_size, hipStream_t stream) {
    Args a;
    a.x = (const char*)d_in[0]; a.k = (const char*)d_in[1]; a.v = (const char*)d_in[2]; a.wq = (const char*)d_in[3]; a.wo = (const char*)d_in[4];
    a.w1 = (const char*)d_in[6]; a.b1 = (const char*)d_in[7]; a.w2 = (const char*)d_in[8]; a.b2 = (const char*)d_in[9]; a.gw = (const char*)d_in[10];
    a.out = (char*)d_out; a.ws = (char*)d_ws + (WSOFF ? ws_size - (8u << 20) : 0);
    gated_mix_kernel<<<256, 256, 0, stream>>>(a);
}
```

```cpp
#include <hip/hip_runtime.h>
#include <stdint.h>

typedef unsigned u32;
typedef unsigned long long u64;
typedef u32 u32x4 __attribute__((ext_vector_type(4)));
typedef float f32x4 __attribute__((ext_vector_type(4)));
typedef __bf16 bf16x8 __attribute__((ext_vector_type(8)));
typedef __bf16 bf16x2 __attribute__((ext_vector_type(2)));
#define GAS __attribute__((address_space(1)))
#define LAS __attribute__((address_space(3)))
#define SB() __builtin_amdgcn_sched_barrier(0)
#define MEMBAR() asm volatile("" ::: "memory")

#define GATHER_AT 9
#define VHOLD 0
#define DELAY_ML 8
#define DELAY_O 8
#define DELAY_Y 8
#define WO_FIRST 0
#define DELAY_F 8
#define F_TRIG 4
#define K0 32
#define REL_F 25
#define REL_T 9
#define VWIN 32
#define PRIME 0
#define PREW 0
#define WSOFF 0

constexpr int SK = 65536, WIN = 4096;
constexpr u32 SPIN_CAP = 1u << 16;
constexpr u32 WS_EPOCH = 0;
constexpr u32 WS_Q = 4096;
constexpr u32 WS_ML = WS_Q + 8 * 16384;
constexpr u32 WS_F = WS_ML + 32768;
constexpr u32 WS_O = WS_F + 8 * 896 * 16;
constexpr u32 WS_Y = WS_O + (1u << 19);
constexpr u32 WS_END = WS_Y + (1u << 18);
constexpr u32 WS_STAMP = 4u << 20;
constexpr int L_FLAG = 0;
constexpr int L_DUMMY = 48;
constexpr int L_A = 64;
constexpr int L_W = 384;
constexpr int L_Z = 128;
constexpr int L_Q = 512;
constexpr int L_P = L_Q + 1280;
constexpr int L_END = L_P + 1280;

struct Args {
    const char* x; const char* k; const char* v; const char* wq; const char* wo;
    const char* w1; const char* b1; const char* w2; const char* b2; const char* gw;
    char* out; char* ws;
};

#define ST(i)
#define CNT(i, n)
#define STF(i, n)
#define ST_DECL
#define ST_OUT()

__device__ __forceinline__ u32x4 ldnt(const char* p) { return __builtin_nontemporal_load((const GAS u32x4*)p); }
typedef __amdgpu_buffer_rsrc_t rsrc_t;
__device__ __forceinline__ rsrc_t mk(const char* p, u32 n) { return __builtin_amdgcn_make_buffer_rsrc((void*)p, 0, n, 0x00020000); }
__device__ __forceinline__ u32x4 bnt(rsrc_t r, u32 v, u32 s) { return __builtin_amdgcn_raw_buffer_load_b128(r, v, s, 2); }
__device__ __forceinline__ u32x4 bld(rsrc_t r, u32 v, u32 s) { return __builtin_amdgcn_raw_buffer_load_b128(r, v, s, 16); }
__device__ __forceinline__ void bstu(u32 a, u32 b, u32 tag, rsrc_t r, u32 v, u32 s) { __builtin_amdgcn_raw_buffer_store_b128(u32x4{a, tag, b, tag}, r, v, s, 16); }
__device__ __forceinline__ u32 chk(u32 bad, u32x4 x, u32 tag) { bad |= x[1] ^ tag; asm volatile("" : "+v"(bad)); bad |= x[3] ^ tag; asm volatile("" : "+v"(bad)); return bad; }
__device__ __forceinline__ void bst(float a, float b, u32 tag, rsrc_t r, u32 v, u32 s) { __builtin_amdgcn_raw_buffer_store_b128(u32x4{__float_as_uint(a), tag, __float_as_uint(b), tag}, r, v, s, 16); }
#define PIN(x) asm volatile("" : "+v"(x))
constexpr u32 OOB = 0x80000000u;
constexpr u32 CODE_BYTES = 12800;
__device__ __forceinline__ float dot2(u32 a, u32 b, float c) { return __builtin_amdgcn_fdot2_f32_bf16(__builtin_bit_cast(bf16x2, a), __builtin_bit_cast(bf16x2, b), c, false); }
__device__ __forceinline__ u32 cvtpk(float lo, float hi) { bf16x2 r = {(__bf16)lo, (__bf16)hi}; return __builtin_bit_cast(u32, r); }
template <int CTRL> __device__ __forceinline__ float dpp(float x) { return __builtin_bit_cast(float, __builtin_amdgcn_mov_dpp(__builtin_bit_cast(int, x), CTRL, 0xf, 0xf, true)); }
constexpr int XOR1 = 0xB1, XOR2 = 0x4E, ROR4 = 0x124, ROR8 = 0x128;
__device__ __forceinline__ float sw16_add(float a, float b) { auto s = __builtin_amdgcn_permlane16_swap(__float_as_uint(a), __float_as_uint(b), false, false); return __uint_as_float(s[0]) + __uint_as_float(s[1]); }
__device__ __forceinline__ float sw32_add(float a, float b) { auto s = __builtin_amdgcn_permlane32_swap(__float_as_uint(a), __float_as_uint(b), false, false); return __uint_as_float(s[0]) + __uint_as_float(s[1]); }
__device__ __forceinline__ float fmx(float a, float b) { return __builtin_elementwise_maximum(a, b); }
__device__ __forceinline__ float sw16_max(float a) { auto s = __builtin_amdgcn_permlane16_swap(__float_as_uint(a), __float_as_uint(a), false, false); return fmx(__uint_as_float(s[0]), __uint_as_float(s[1])); }
__device__ __forceinline__ float sw32_max(float a) { auto s = __builtin_amdgcn_permlane32_swap(__float_as_uint(a), __float_as_uint(a), false, false); return fmx(__uint_as_float(s[0]), __uint_as_float(s[1])); }
__device__ __forceinline__ f32x4 mfma(u32x4 a, u32x4 b, f32x4 c) { return __builtin_amdgcn_mfma_f32_16x16x32_bf16(__builtin_bit_cast(bf16x8, a), __builtin_bit_cast(bf16x8, b), c, 0, 0, 0); }
__device__ __forceinline__ float bf_lo(u32 w) { return __uint_as_float(w << 16); }
__device__ __forceinline__ float bf_hi(u32 w) { return __uint_as_float(w & 0xffff0000u); }

__device__ __forceinline__ void transpose8(const u32x4* r, u32x4* t) {
#pragma unroll
    for (int p = 0; p < 4; ++p)
#pragma unroll
        for (int d = 0; d < 4; ++d) {
            t[2 * d][p] = __builtin_amdgcn_perm(r[2 * p + 1][d], r[2 * p][d], 0x05040100u);
            t[2 * d + 1][p] = __builtin_amdgcn_perm(r[2 * p + 1][d], r[2 * p][d], 0x07060302u);
        }
}
__device__ __forceinline__ void row_fma(float* acc, u32x4 w, u32 xpair, int odd) {
    const u32 xl = odd ? xpair >> 16 : xpair & 0xffffu, xh = odd ? xpair & 0xffff0000u : xpair << 16;
#pragma unroll
    for (int d = 0; d < 4; ++d) { acc[2 * d] = dot2(w[d], xl, acc[2 * d]); acc[2 * d + 1] = dot2(w[d], xh, acc[2 * d + 1]); }
}
__device__ __forceinline__ void reduce8(const float* acc, float& u0, float& u1) {
    const float s0 = sw32_add(acc[0], acc[4]), s1 = sw32_add(acc[1], acc[5]), s2 = sw32_add(acc[2], acc[6]), s3 = sw32_add(acc[3], acc[7]);
    u0 = sw16_add(s0, s2); u1 = sw16_add(s1, s3);
    u0 += dpp<ROR8>(u0); u1 += dpp<ROR8>(u1);
}
__device__ __forceinline__ float gate_of(u32x4 x, u32x4 gw) {
    float s = 0.f;
#pragma unroll
    for (int d = 0; d < 4; ++d) s = dot2(x[d], gw[d], s);
    s += dpp<XOR1>(s); s += dpp<XOR2>(s); s += dpp<ROR4>(s); s += dpp<ROR8>(s);
    s = sw16_add(s, s); s = sw32_add(s, s);
    return __builtin_amdgcn_rcpf(1.f + __builtin_amdgcn_exp2f(-1.4426950408889634f * s));
}
template <int OFF> __device__ __forceinline__ void lds_wait() {
    u32 t, n, c;
    asm volatile("s_mov_b32 %1, 0x100000\n\tv_mov_b32 %0, 0\n"
                 "1:\n\tds_read_b32 %0, %0 offset:%3\n\ts_waitcnt lgkmcnt(0)\n\tv_readfirstlane_b32 %2, %0\n\ts_cmp_lg_u32 %2, 0\n\ts_cbranch_scc1 2f\n\t"
                 "s_sleep 1\n\ts_sub_u32 %1, %1, 1\n\ts_cbranch_scc0 1b\n"
                 "2:" : "=&v"(t), "=&s"(n), "=&s"(c) : "n"(OFF) : "memory", "scc");
}
#define BARRIER() do { asm volatile("s_waitcnt lgkmcnt(0)" ::: "memory"); __builtin_amdgcn_s_barrier(); MEMBAR(); } while (0)

__device__ __forceinline__ u64 rt2() { u64 t; SB(); asm volatile("s_memrealtime %0\n\ts_waitcnt lgkmcnt(0)" : "=s"(t) :: "memory"); SB(); return t; }
__global__ void __launch_bounds__(64, 2) mark_post(char* ws, int amp) { const u64 t = rt2();
    __shared__ u32 l_[768]; *(volatile LAS u32*)&l_[threadIdx.x] = 1;
    asm volatile("v_mov_b32 v224, 0" ::: "v224");
    u64 m = 0; for (int i = 0; i < 4; ++i) { u64 v = *(GAS u64*)(ws + (8u << 20) + 64 + ((threadIdx.x & 63) * 4 + i) * 8); m = v > m ? v : m; }
    for (int o = 32; o > 0; o >>= 1) { u64 v = __shfl_xor(m, o); m = v > m ? v : m; }
    u64 d = t - m; if (d > 3000) d = 3000; const u64 te = rt2() + amp * d; while (rt2() < te) __builtin_amdgcn_s_sleep(1);
    if (amp == 12345) asm volatile(".rept 3000\n\ts_nop 0\n\t.endr");
}
__global__ void __launch_bounds__(256, 2) gated_mix_kernel(Args a) {
    __shared__ __attribute__((aligned(16))) char lds_[L_END];
    LAS char* lds = (LAS char*)lds_;
    volatile LAS u32* flag = (volatile LAS u32*)(lds + L_FLAG);
    const int b = blockIdx.x, g = b & 7, s = b >> 3, lane = threadIdx.x & 63;
    int wv = __builtin_amdgcn_readfirstlane(threadIdx.x >> 6);
    ST_DECL; ST(0);
    u64 pc; asm volatile("s_getpc_b64 %0" : "=s"(pc));
    if (threadIdx.x < L_Q / 4) *(volatile LAS u32*)(lds + threadIdx.x * 4) = 0;
    asm volatile("" :: "s"(a.x), "s"(a.k), "s"(a.v), "s"(a.wq), "s"(a.wo), "s"(a.w1), "s"(a.b1), "s"(a.w2), "s"(a.b2), "s"(a.gw), "s"(a.out), "s"(a.ws));
    BARRIER();
    const rsrc_t ws = mk(a.ws, WS_END);
    const u32 kvoff = (u32)(g * SK + (SK - WIN) + s * 128) * 256u;
    u32 cw = __builtin_amdgcn_raw_buffer_load_b32(mk((const char*)(pc & ~255ull), wv == 3 ? CODE_BYTES : 0), (lane & 15) * 128 + (lane >> 4) * 8192, (s & 3) * 2048, 0);
    SB();
    const int rho = 8 * g + (s >> 2), cq = s & 3;

    if (wv == 0) {
        const int kq = s & 3, r8 = lane >> 3;
        u32 tag = __builtin_amdgcn_raw_buffer_load_b32(ws, 0, WS_EPOCH, 16);
        u32x4 xr[2], w[16], kf[32];
        const char* xp = a.x + (kq * 128 + r8 * 16) * 2;
        xr[0] = *(const GAS u32x4*)xp; xr[1] = *(const GAS u32x4*)(xp + 16);
        const rsrc_t rq = mk(a.wq + kq * (128 * 8192) + g * 1024 + (s >> 2) * 128, 128 * 8192);
        const u32 wo_ = r8 * (16 * 8192) + (lane & 7) * 16;
#pragma unroll
        for (int i = 0; i < 16; ++i) { w[i] = bnt(rq, wo_, i * 8192); SB(); }
        const int key3 = (lane >> 1) & 7;
        const u32 ko = key3 * 256 + (2 * (lane >> 4) + (lane & 1)) * 16;
        const rsrc_t rk = mk(a.k + kvoff, 32768);
#pragma unroll
        for (int i = 0; i < K0; ++i) kf[i] = bnt(rk, ko + ((i >> 1) & 1) * 2048 + (i & 1) * 128, (i >> 2) * 4096);
        SB();
        tag += 1;
        flag[10] = tag;
        ST(1);
        const int hd = (lane >> 2) & 3, xb = (lane >> 1) & 1, yb = lane & 1;
        u32 qsa = (lane & 8) ? OOB : ((s >> 2) * 32 + 4 * (lane & 7) + 2 * (lane >> 5) + ((lane >> 4) & 1)) * 16;
        u32 qa0 = L_Q + hd * 320 + (2 * (lane >> 4) + yb) * 16, qa1 = xb ? qa0 : L_Z;
        qa0 = xb ? L_Z : qa0;
        u32 pwa = L_P + hd * 320 + (2 * xb + (lane >> 5)) * 64 + (2 * ((lane >> 4) & 1) + yb) * 16;
        u32 mla = (lane & 0x23) ? OOB : hd * 16 + (lane & 16) * 4;
        u32 l16 = lane * 16;
        PIN(qsa); PIN(qa0); PIN(qa1); PIN(pwa); PIN(mla); PIN(l16);
        SB();
        float acc[8] = {0.f, 0.f, 0.f, 0.f, 0.f, 0.f, 0.f, 0.f};
#pragma unroll
        for (int i = 0; i < 16; ++i) row_fma(acc, w[i], xr[i >> 3][(i >> 1) & 3], i & 1);
        float u0, u1;
        reduce8(acc, u0, u1);
        ST(2);
        bst(u0, u1, tag, ws, qsa, WS_Q + g * 16384 + kq * 4096);
        SB();
#pragma unroll
        for (int i = K0; i < 32; ++i) {
            if ((i & 3) == 0) { asm volatile("s_waitcnt vmcnt(%0)" :: "n"(K0 - 3) : "memory"); SB(); }
            kf[i] = bnt(rk, ko + ((i >> 1) & 1) * 2048 + (i & 1) * 128, (i >> 2) * 4096);
            SB();
        }
        asm volatile("s_waitcnt vmcnt(%0)" :: "n"(REL_F) : "memory");
        flag[0] = 1;
        asm volatile("s_waitcnt vmcnt(%0)" :: "n"(GATHER_AT) : "memory");
        ST(3);
        u32x4 qg[16];
#pragma nounroll
        for (u32 n = 0; n < SPIN_CAP; ++n) {
            MEMBAR();
#pragma unroll
            for (int i = 0; i < 16; ++i) qg[i] = bld(ws, l16 + (i >> 2) * 1024, WS_Q + g * 16384 + (i & 3) * 4096);
            SB();
            flag[3] = 1;
            SB();
            u32 bad = 0;
#pragma unroll
            for (int i = 0; i < 16; ++i) bad = chk(bad, qg[i], tag);
            CNT(8, n); STF(7, n);
            if (__builtin_amdgcn_ballot_w64(bad != 0) == 0) break;
        }
        ST(4);
#pragma unroll
        for (int t = 0; t < 4; ++t) {
            const float q0 = (__uint_as_float(qg[4 * t][0]) + __uint_as_float(qg[4 * t + 1][0])) + (__uint_as_float(qg[4 * t + 2][0]) + __uint_as_float(qg[4 * t + 3][0]));
            const float q1 = (__uint_as_float(qg[4 * t][2]) + __uint_as_float(qg[4 * t + 1][2])) + (__uint_as_float(qg[4 * t + 2][2]) + __uint_as_float(qg[4 * t + 3][2]));
            *(volatile LAS u32*)(lds + L_Q + t * 320 + lane * 4) = cvtpk(q0, q1);
        }
        u32x4 qf[2][2];
        qf[0][0] = *(volatile LAS u32x4*)(lds + qa0); qf[0][1] = *(volatile LAS u32x4*)(lds + qa0 + 128);
        qf[1][0] = *(volatile LAS u32x4*)(lds + qa1); qf[1][1] = *(volatile LAS u32x4*)(lds + qa1 + 128);
        SB();
        float sc[8];
#pragma unroll
        for (int n = 0; n < 8; ++n) {
            f32x4 d = {0.f, 0.f, 0.f, 0.f};
            d = mfma(kf[4 * n], qf[0][0], d); d = mfma(kf[4 * n + 1], qf[0][1], d);
            d = mfma(kf[4 * n + 2], qf[1][0], d); d = mfma(kf[4 * n + 3], qf[1][1], d);
            const float p = yb ? d[3] : d[0], q = yb ? d[1] : d[2];
            sc[n] = p + dpp<XOR1>(q);
        }
        ST(5);
        constexpr float C2 = 0.08838834764831845f * 1.4426950408889634f;
        float m = fmx(fmx(fmx(sc[0], sc[1]), fmx(sc[2], sc[3])), fmx(fmx(sc[4], sc[5]), fmx(sc[6], sc[7])));
        m = fmx(m, dpp<XOR1>(m)); m = fmx(m, dpp<XOR2>(m)); m = sw16_max(m); m = sw32_max(m);
        m *= C2;
        float p[8];
#pragma unroll
        for (int n = 0; n < 8; ++n) p[n] = __builtin_amdgcn_exp2f(fmaf(sc[n], C2, -m));
        *(volatile LAS u32x4*)(lds + pwa) = u32x4{cvtpk(p[0], p[1]), cvtpk(p[2], p[3]), cvtpk(p[4], p[5]), cvtpk(p[6], p[7])};
        flag[5] = 1;
        SB();
        ST(6);
        float l = ((p[0] + p[1]) + (p[2] + p[3])) + ((p[4] + p[5]) + (p[6] + p[7]));
        l += dpp<XOR1>(l); l += dpp<XOR2>(l); l = sw16_add(l, l); l = sw32_add(l, l);
        bst(m, l, tag, ws, mla, WS_ML + g * 4096 + s * 128);
        ST_OUT();
    }
    asm volatile("" : "+s"(wv));
    if (wv == 1) {
        u32 oa = ((lane >> 4) * 2 + ((lane >> 3) & 1)) * 8192 + (lane & 7) * 16;
        u32 pa = L_P + ((lane >> 2) & 3) * 320 + (lane >> 4) * 16;
        u32 l16 = lane * 16;
        PIN(oa); PIN(pa); PIN(l16);
        const rsrc_t rv = mk(a.v + kvoff, 32768);
        if (PREW & 2) { bst(0.f, 0.f, 0, ws, oa, WS_O + g * 65536 + s * 256); bst(0.f, 0.f, 0, ws, oa + 128, WS_O + g * 65536 + s * 256); }
        lds_wait<12>();
        ST(1);
        u32x4 v[32];
#pragma unroll
        for (int i = 0; i < 32; ++i) {
            if (i >= VWIN && (i & 3) == 0) { SB(); asm volatile("s_waitcnt vmcnt(%0)" :: "n"(VWIN - 4) : "memory"); SB(); }
            v[i] = bnt(rv, l16 + (i >> 3) * 1024, (i & 7) * 4096);
        }
        SB();
        flag[4] = 1;
        u32 tag = flag[10]; PIN(tag);
        ST(2);
        u32x4 t0[8], t1[8], t2[8], t3[8], pf[4];
        transpose8(v, t0);
        transpose8(v + 8, t1);
        transpose8(v + 16, t2);
#pragma unroll
        for (int i = 0; i < 8; ++i) { PIN(t0[i]); PIN(t1[i]); PIN(t2[i]); }
        SB();
#pragma nounroll
        for (u32 n = 0; n < SPIN_CAP * 16; ++n) {
            const u32 f = flag[5];
#pragma unroll
            for (int i = 0; i < 4; ++i) pf[i] = *(volatile LAS u32x4*)(lds + pa + i * 64);
            if (__builtin_amdgcn_readfirstlane(f)) break;
        }
        ST(3);
        f32x4 o[8];
#pragma unroll
        for (int e = 0; e < 8; ++e) { o[e] = mfma(pf[0], t0[e], f32x4{0.f, 0.f, 0.f, 0.f}); }
#pragma unroll
        for (int e = 0; e < 8; ++e) o[e] = mfma(pf[1], t1[e], o[e]);
#pragma unroll
        for (int e = 0; e < 8; ++e) o[e] = mfma(pf[2], t2[e], o[e]);
        transpose8(v + 24, t3);
        ST(4);
#pragma unroll
        for (int e = 0; e < 8; ++e) o[e] = mfma(pf[3], t3[e], o[e]);
#pragma unroll
        for (int k = 0; k < 2; ++k) bstu(cvtpk(o[4 * k][0], o[4 * k + 1][0]), cvtpk(o[4 * k + 2][0], o[4 * k + 3][0]), tag, ws, oa + k * 128, WS_O + g * 65536 + s * 256);
        flag[6] = 1;
        ST_OUT();
    }
    asm volatile("" : "+s"(wv));
    if (wv == 2) {
        const int bb = b < 224 ? b : b - 224, kgrp = bb / 28, cgrp = bb - kgrp * 28, r8 = lane >> 3;
        const int j = g * 32 + s, col = 128 * (j >> 6) + 8 * (j & 15) + 2 * ((j >> 4) & 3);
        const u32x4 xa = *(const GAS u32x4*)(a.x + lane * 16), gw = *(const GAS u32x4*)(a.gw + lane * 16);
        const u32x4 xr = *(const GAS u32x4*)(a.x + (kgrp * 64 + r8 * 8) * 2);
        const int pr = (lane & 15) < 14 ? (lane & 15) : 13;
        const u32 b1w = *(const GAS u32*)(a.b1 + (rho * 14 + pr) * 4);
        const u32 b2w = __builtin_amdgcn_raw_buffer_load_b32(mk(a.b2, 1024), 0, col * 2, 0);
        SB();
        const rsrc_t r1 = mk(a.w1 + kgrp * (64 * 3584) + cgrp * 128, b < 224 ? 64 * 3584 - cgrp * 128 : 0);
        u32 wa = r8 * (8 * 3584) + (lane & 7) * 16;
        u32 fsa = ((lane & 8) || b >= 224) ? OOB : (cgrp * 32 + 4 * (lane & 7) + 2 * (lane >> 5) + ((lane >> 4) & 1)) * 16;
        u32 fa = (lane >> 4) * 14336 + (rho * 14 + pr) * 16;
        u32 ha = lane < 14 ? L_A + lane * 4 : L_DUMMY;
        u32 ya = lane * 1024, outa = (lane & 31) ? OOB : (lane >> 4), epa = (b | lane) ? OOB : 0;
        u32 mla = (lane >> 4) * 1024, wla = L_W + (lane >> 4) * 16;
        PIN(mla); PIN(wla);
        PIN(wa); PIN(fsa); PIN(fa); PIN(ha); PIN(ya); PIN(outa); PIN(epa);
        lds_wait<0>();
        u32x4 w[8];
#pragma unroll
        for (int i = 0; i < 8; ++i) w[i] = bnt(r1, wa, i * 3584);
        SB();
        ST(2);
        float g1 = 1.f - gate_of(xa, gw), bb0 = bf_lo(b1w), bb1 = bf_hi(b1w);
        float bias = g1 * (lane < 32 ? bf_lo(b2w) : bf_hi(b2w));
        u32 tag = flag[10];
        PIN(g1); PIN(bb0); PIN(bb1); PIN(bias); PIN(tag);
        SB();
        float acc[8] = {0.f, 0.f, 0.f, 0.f, 0.f, 0.f, 0.f, 0.f};
#pragma unroll
        for (int i = 0; i < 8; ++i) row_fma(acc, w[i], xr[i >> 1], i & 1);
        float u0, u1;
        reduce8(acc, u0, u1);
        bst(u0, u1, tag, ws, fsa, WS_F + kgrp * 14336);
        SB();
        ST(3);
        lds_wait<F_TRIG * 4>();
        __builtin_amdgcn_s_sleep(DELAY_F);
        u32x4 f0 = bld(ws, fa, WS_F), f1 = bld(ws, fa, WS_F + 4 * 14336);
        SB();
        lds_wait<20>();
        __builtin_amdgcn_s_sleep(DELAY_ML);
        u32x4 ml[8];
#pragma unroll
        for (int k = 0; k < 8; ++k) ml[k] = bld(ws, mla + k * 128, WS_ML + g * 4096 + (s >> 3) * 16);
        SB();
        ST(10);
        if (__builtin_expect(__builtin_amdgcn_ballot_w64(chk(chk(0, f0, tag), f1, tag) != 0) != 0, 0)) {
#pragma nounroll
            for (u32 n = 1; n < SPIN_CAP; ++n) {
                MEMBAR();
                f0 = bld(ws, fa, WS_F); f1 = bld(ws, fa, WS_F + 4 * 14336);
                CNT(8, n);
                if (__builtin_amdgcn_ballot_w64(chk(chk(0, f0, tag), f1, tag) != 0) == 0) break;
            }
        }
        ST(4);
        float h0 = __uint_as_float(f0[0]) + __uint_as_float(f1[0]), h1 = __uint_as_float(f0[2]) + __uint_as_float(f1[2]);
        h0 = sw16_add(h0, h0); h1 = sw16_add(h1, h1);
        h0 = sw32_add(h0, h0) + bb0; h1 = sw32_add(h1, h1) + bb1;
        constexpr float GC = -2.f * 0.7978845608028654f * 1.4426950408889634f;
        h0 = h0 * __builtin_amdgcn_rcpf(1.f + __builtin_amdgcn_exp2f(GC * fmaf(0.044715f * h0 * h0, h0, h0)));
        h1 = h1 * __builtin_amdgcn_rcpf(1.f + __builtin_amdgcn_exp2f(GC * fmaf(0.044715f * h1 * h1, h1, h1)));
        *(volatile LAS u32*)(lds + ha) = cvtpk(h0 * g1, h1 * g1);
        flag[7] = 1;
        SB();
        ST(5);
        {
            u32 bad = 0;
#pragma unroll
            for (int k = 0; k < 8; ++k) bad = chk(bad, ml[k], tag);
            if (__builtin_expect(__builtin_amdgcn_ballot_w64(bad != 0) != 0, 0)) {
#pragma nounroll
                for (u32 n = 1; n < SPIN_CAP; ++n) {
                    MEMBAR();
#pragma unroll
                    for (int k = 0; k < 8; ++k) ml[k] = bld(ws, mla + k * 128, WS_ML + g * 4096 + (s >> 3) * 16);
                    bad = 0;
#pragma unroll
                    for (int k = 0; k < 8; ++k) bad = chk(bad, ml[k], tag);
                    if (__builtin_amdgcn_ballot_w64(bad != 0) == 0) break;
                }
            }
        }
        float M = fmx(fmx(fmx(__uint_as_float(ml[0][0]), __uint_as_float(ml[1][0])), fmx(__uint_as_float(ml[2][0]), __uint_as_float(ml[3][0]))),
                      fmx(fmx(__uint_as_float(ml[4][0]), __uint_as_float(ml[5][0])), fmx(__uint_as_float(ml[6][0]), __uint_as_float(ml[7][0]))));
        M = sw16_max(M); M = sw32_max(M);
        float wg[8], L = 0.f;
#pragma unroll
        for (int k = 0; k < 8; ++k) { wg[k] = __builtin_amdgcn_exp2f(__uint_as_float(ml[k][0]) - M); L = fmaf(wg[k], __uint_as_float(ml[k][2]), L); }
        L = sw16_add(L, L); L = sw32_add(L, L);
        const float sc = (1.f - g1) * __builtin_amdgcn_rcpf(L);
        *(volatile LAS u32x4*)(lds + wla) = u32x4{cvtpk(wg[0] * sc, wg[1] * sc), cvtpk(wg[2] * sc, wg[3] * sc), cvtpk(wg[4] * sc, wg[5] * sc), cvtpk(wg[6] * sc, wg[7] * sc)};
        flag[9] = 1;
        SB();
        ST(1);
        u32x4 y;
        if (PRIME & 2) { y = bld(ws, ya, WS_Y + (j >> 6) * 65536 + (j & 63) * 16); PIN(y); }
        lds_wait<32>();
        __builtin_amdgcn_s_sleep(DELAY_Y);
#pragma nounroll
        for (u32 n = 0; n < SPIN_CAP; ++n) {
            MEMBAR();
            y = bld(ws, ya, WS_Y + (j >> 6) * 65536 + (j & 63) * 16);
            const u32 bad = chk(0, y, tag);
            CNT(9, n); STF(7, n);
            if (__builtin_amdgcn_ballot_w64(bad != 0) == 0) break;
        }
        ST(6);
        float r = sw32_add(__uint_as_float(y[0]), __uint_as_float(y[2]));
        r = sw16_add(r, r); r += dpp<ROR8>(r); r += dpp<ROR4>(r); r += dpp<XOR2>(r); r += dpp<XOR1>(r);
        r += bias;
        __builtin_amdgcn_raw_buffer_store_b16((unsigned short)cvtpk(r, r), mk(a.out + col * 2, 4), outa, 0, 0);
        __builtin_amdgcn_raw_buffer_store_b32(tag, ws, epa, WS_EPOCH, 16);
        if (lane == 0) *(GAS u64*)(a.ws + (8u << 20) + 64 + b * 8) = rt2();
        ST_OUT();
    }
    asm volatile("" : "+s"(wv));
    if (wv == 3) {
        const int kg = lane >> 4;
        const rsrc_t r2 = mk(a.w2 + rho * (28 * 1024) + cq * 256, 28 * 1024 - cq * 256);
        const rsrc_t ro = mk(a.wo + rho * (64 * 1024) + cq * 256, 64 * 1024 - cq * 256);
        u32 w2a = kg * 8192 + (lane & 15) * 16, w2b = kg == 3 ? w2a - 4096 : w2a;
        u32 woa = (32 * (kg & 1) + 4 * (kg >> 1)) * 1024 + (lane & 15) * 16;
        u32 oga = kg * 2048 + (lane & 15) * 16, l16 = lane * 16, aa = L_A + kg * 16;
        PIN(w2a); PIN(w2b); PIN(woa); PIN(oga); PIN(l16); PIN(aa);
        if (PREW & 8) bst(0.f, 0.f, 0, ws, l16, WS_Y + cq * 65536 + rho * 1024);
        lds_wait<0>();
        u32x4 r[8], u[16], t2[8], ta[8], tb[8];
#pragma unroll
        for (int i = 0; i < 8; ++i) r[i] = bnt(r2, (i < 4 ? w2a : w2b) + (i & 3) * 1024, (i >> 2) * 4096);
        SB();
        ST(1);
        lds_wait<16>();
#pragma unroll
        for (int i = 0; i < 16; ++i) u[i] = bnt(ro, woa + (2 * (i >> 3) + ((i >> 2) & 1)) * 1024, (i & 3) * 8192);
        SB();
        ST(2);
        u32 tag = flag[10];
        PIN(tag); PIN(cw);
        transpose8(r, t2);
#pragma unroll
        for (int i = 0; i < 8; ++i) PIN(t2[i]);
        lds_wait<28>();
        const u32x4 af = *(volatile LAS u32x4*)(lds + aa);
        f32x4 d[8];
#pragma unroll
        for (int e = 0; e < 8; ++e) { d[e] = mfma(af, t2[e], f32x4{0.f, 0.f, 0.f, 0.f}); PIN(d[e]); }
        SB();
        ST(3);
        transpose8(u, ta);
        transpose8(u + 8, tb);
#pragma unroll
        for (int i = 0; i < 8; ++i) { PIN(ta[i]); PIN(tb[i]); }
        ST(5);
        lds_wait<24>();
        { const u64 t0 = __builtin_amdgcn_s_memtime(); lds_wait<36>(); while (__builtin_amdgcn_s_memtime() - t0 < (u64)(DELAY_O * 64)) {} }
        ST(4);
        u32x4 og[8];
#pragma unroll
        for (int k = 0; k < 8; ++k) og[k] = bld(ws, oga + k * 256, WS_O + rho * 8192);
        SB();
        u32x4 wf = *(volatile LAS u32x4*)(lds + aa + (L_W - L_A));
        PIN(wf);
        SB();
        {
            u32 bad = 0;
#pragma unroll
            for (int k = 0; k < 8; ++k) bad = chk(bad, og[k], tag);
            ST(10);
            if (__builtin_expect(__builtin_amdgcn_ballot_w64(bad != 0) != 0, 0)) {
#pragma nounroll
                for (u32 n = 1; n < SPIN_CAP; ++n) {
                    MEMBAR();
#pragma unroll
                    for (int k = 0; k < 8; ++k) og[k] = bld(ws, oga + k * 256, WS_O + rho * 8192);
                    bad = 0;
#pragma unroll
                    for (int k = 0; k < 8; ++k) bad = chk(bad, og[k], tag);
                    CNT(9, n);
                    if (__builtin_amdgcn_ballot_w64(bad != 0) == 0) break;
                }
            }
        }
        ST(7);
        f32x4 c[4];
#pragma unroll
        for (int e = 0; e < 4; ++e) {
            u32x4 ot;
#pragma unroll
            for (int p = 0; p < 4; ++p) ot[p] = __builtin_amdgcn_perm(og[2 * p + 1][e & 2], og[2 * p][e & 2], (e & 1) ? 0x07060302u : 0x05040100u);
            c[e] = mfma(ot, wf, f32x4{0.f, 0.f, 0.f, 0.f});
        }
        const u32x4 a0 = {cvtpk(c[0][0], c[0][1]), cvtpk(c[0][2], c[0][3]), cvtpk(c[1][0], c[1][1]), cvtpk(c[1][2], c[1][3])};
        const u32x4 a1 = {cvtpk(c[2][0], c[2][1]), cvtpk(c[2][2], c[2][3]), cvtpk(c[3][0], c[3][1]), cvtpk(c[3][2], c[3][3])};
#pragma unroll
        for (int e = 0; e < 8; ++e) d[e] = mfma(a0, ta[e], d[e]);
#pragma unroll
        for (int e = 0; e < 8; ++e) d[e] = mfma(a1, tb[e], d[e]);
        const bool k0 = kg & 1, k1 = kg & 2;
        const float ya0 = k0 ? d[2][0] : d[0][0], yb0 = k0 ? d[6][0] : d[4][0], ya1 = k0 ? d[3][0] : d[1][0], yb1 = k0 ? d[7][0] : d[5][0];
        const float y0 = k1 ? yb0 : ya0, y1 = k1 ? yb1 : ya1;
        bst(y0, y1, tag, ws, l16, WS_Y + cq * 65536 + rho * 1024);
        flag[8] = 1;
        ST_OUT();
    }
}

extern "C" void kernel_launch(void* const* d_in, const int* in_sizes, int n_in, void* d_out, int out_size, void* d_ws, size_t ws_size, hipStream_t stream) {
    Args a;
    a.x = (const char*)d_in[0]; a.k = (const char*)d_in[1]; a.v = (const char*)d_in[2]; a.wq = (const char*)d_in[3]; a.wo = (const char*)d_in[4];
    a.w1 = (const char*)d_in[6]; a.b1 = (const char*)d_in[7]; a.w2 = (const char*)d_in[8]; a.b2 = (const char*)d_in[9]; a.gw = (const char*)d_in[10];
    a.out = (char*)d_out; a.ws = (char*)d_ws + (WSOFF ? ws_size - (8u << 20) : 0);
    gated_mix_kernel<<<256, 256, 0, stream>>>(a);
    mark_post<<<1, 64, 0, stream>>>(a.ws, 10);

}
```

```cpp
#include <hip/hip_runtime.h>
#include <stdint.h>

typedef unsigned u32;
typedef unsigned long long u64;
typedef u32 u32x4 __attribute__((ext_vector_type(4)));
typedef float f32x4 __attribute__((ext_vector_type(4)));
typedef __bf16 bf16x8 __attribute__((ext_vector_type(8)));
typedef __bf16 bf16x2 __attribute__((ext_vector_type(2)));
#define GAS __attribute__((address_space(1)))
#define LAS __attribute__((address_space(3)))
#define SB() __builtin_amdgcn_sched_barrier(0)
#define MEMBAR() asm volatile("" ::: "memory")

#define GATHER_AT 1
#define VHOLD 16
#define DELAY_ML 20
#define DELAY_O 16
#define DELAY_Y 22
#define WO_FIRST 0
#define DELAY_F 8
#define F_TRIG 5
#define K0 32
#define REL_F 29
#define REL_T 9
#define VWIN 32
#define PRIME 0
#define REL_V 13
#define PREY 0
#define W2_LATE 1
#define GATE_W 1
#define WW 28
#define GPOS 28
#define NTP 0
#define WW2 WW
#define PSW 72
#define PREW 0
#define WSOFF 0

constexpr int SK = 65536, WIN = 4096;
constexpr u32 SPIN_CAP = 1u << 16;
constexpr u32 WS_EPOCH = 0;
constexpr u32 WS_Q = 4096;
constexpr u32 WS_ML = WS_Q + 8 * 16384;
constexpr u32 WS_F = WS_ML + 32768;
constexpr u32 WS_O = WS_F + 64 * 2048;
constexpr u32 WS_Y = WS_O + (1u << 19);
constexpr u32 WS_END = WS_Y + (1u << 18);
constexpr u32 WS_STAMP = 4u << 20;
constexpr int L_FLAG = 0;
constexpr int L_DUMMY = 60;
constexpr int L_A = 64;
constexpr int L_W = 384;
constexpr int L_Z = 128;
constexpr int L_Q = 512;
constexpr int L_P = L_Q + 1280;
constexpr int L_END = L_P + 1280;

struct Args {
    const char* x; const char* k; const char* v; const char* wq; const char* wo;
    const char* w1; const char* b1; const char* w2; const char* b2; const char* gw;
    char* out; char* ws;
};

#define ST(i)
#define CNT(i, n)
#define STF(i, n)
#define ST_DECL
#define ST_OUT()

__device__ __forceinline__ u32x4 ldnt(const char* p) { return __builtin_nontemporal_load((const GAS u32x4*)p); }
typedef __amdgpu_buffer_rsrc_t rsrc_t;
__device__ __forceinline__ rsrc_t mk(const char* p, u32 n) { return __builtin_amdgcn_make_buffer_rsrc((void*)p, 0, n, 0x00020000); }
__device__ __forceinline__ u32x4 bnt(rsrc_t r, u32 v, u32 s) { return __builtin_amdgcn_raw_buffer_load_b128(r, v, s, 2); }
__device__ __forceinline__ u32x4 bld(rsrc_t r, u32 v, u32 s) { return __builtin_amdgcn_raw_buffer_load_b128(r, v, s, 16); }
__device__ __forceinline__ void bstu(u32 a, u32 b, u32 tag, rsrc_t r, u32 v, u32 s) { __builtin_amdgcn_raw_buffer_store_b128(u32x4{a, tag, b, tag}, r, v, s, 16); }
__device__ __forceinline__ u32 chk(u32 bad, u32x4 x, u32 tag) { bad |= x[1] ^ tag; asm volatile("" : "+v"(bad)); bad |= x[3] ^ tag; asm volatile("" : "+v"(bad)); return bad; }
__device__ __forceinline__ void bst(float a, float b, u32 tag, rsrc_t r, u32 v, u32 s) { __builtin_amdgcn_raw_buffer_store_b128(u32x4{__float_as_uint(a), tag, __float_as_uint(b), tag}, r, v, s, 16); }
#define PIN(x) asm volatile("" : "+v"(x))
constexpr u32 OOB = 0x80000000u;
constexpr u32 CODE_BYTES = 12800;
__device__ __forceinline__ float dot2(u32 a, u32 b, float c) { return __builtin_amdgcn_fdot2_f32_bf16(__builtin_bit_cast(bf16x2, a), __builtin_bit_cast(bf16x2, b), c, false); }
__device__ __forceinline__ u32 cvtpk(float lo, float hi) { bf16x2 r = {(__bf16)lo, (__bf16)hi}; return __builtin_bit_cast(u32, r); }
template <int CTRL> __device__ __forceinline__ float dpp(float x) { return __builtin_bit_cast(float, __builtin_amdgcn_mov_dpp(__builtin_bit_cast(int, x), CTRL, 0xf, 0xf, true)); }
constexpr int XOR1 = 0xB1, XOR2 = 0x4E, ROR4 = 0x124, ROR8 = 0x128;
__device__ __forceinline__ float sw16_add(float a, float b) { auto s = __builtin_amdgcn_permlane16_swap(__float_as_uint(a), __float_as_uint(b), false, false); return __uint_as_float(s[0]) + __uint_as_float(s[1]); }
__device__ __forceinline__ float sw32_add(float a, float b) { auto s = __builtin_amdgcn_permlane32_swap(__float_as_uint(a), __float_as_uint(b), false, false); return __uint_as_float(s[0]) + __uint_as_float(s[1]); }
__device__ __forceinline__ float fmx(float a, float b) { return __builtin_elementwise_maximum(a, b); }
__device__ __forceinline__ float sw16_max(float a) { auto s = __builtin_amdgcn_permlane16_swap(__float_as_uint(a), __float_as_uint(a), false, false); return fmx(__uint_as_float(s[0]), __uint_as_float(s[1])); }
__device__ __forceinline__ float sw32_max(float a) { auto s = __builtin_amdgcn_permlane32_swap(__float_as_uint(a), __float_as_uint(a), false, false); return fmx(__uint_as_float(s[0]), __uint_as_float(s[1])); }
__device__ __forceinline__ f32x4 mfma(u32x4 a, u32x4 b, f32x4 c) { return __builtin_amdgcn_mfma_f32_16x16x32_bf16(__builtin_bit_cast(bf16x8, a), __builtin_bit_cast(bf16x8, b), c, 0, 0, 0); }
__device__ __forceinline__ float bf_lo(u32 w) { return __uint_as_float(w << 16); }
__device__ __forceinline__ float bf_hi(u32 w) { return __uint_as_float(w & 0xffff0000u); }

__device__ __forceinline__ void transpose8(const u32x4* r, u32x4* t) {
#pragma unroll
    for (int p = 0; p < 4; ++p)
#pragma unroll
        for (int d = 0; d < 4; ++d) {
            t[2 * d][p] = __builtin_amdgcn_perm(r[2 * p + 1][d], r[2 * p][d], 0x05040100u);
            t[2 * d + 1][p] = __builtin_amdgcn_perm(r[2 * p + 1][d], r[2 * p][d], 0x07060302u);
        }
}
__device__ __forceinline__ void row_fma(float* acc, u32x4 w, u32 xpair, int odd) {
    const u32 xl = odd ? xpair >> 16 : xpair & 0xffffu, xh = odd ? xpair & 0xffff0000u : xpair << 16;
#pragma unroll
    for (int d = 0; d < 4; ++d) { acc[2 * d] = dot2(w[d], xl, acc[2 * d]); acc[2 * d + 1] = dot2(w[d], xh, acc[2 * d + 1]); }
}
__device__ __forceinline__ void reduce8(const float* acc, float& u0, float& u1) {
    const float s0 = sw32_add(acc[0], acc[4]), s1 = sw32_add(acc[1], acc[5]), s2 = sw32_add(acc[2], acc[6]), s3 = sw32_add(acc[3], acc[7]);
    u0 = sw16_add(s0, s2); u1 = sw16_add(s1, s3);
    u0 += dpp<ROR8>(u0); u1 += dpp<ROR8>(u1);
}
__device__ __forceinline__ float gate_of(u32x4 x, u32x4 gw) {
    float s = 0.f;
#pragma unroll
    for (int d = 0; d < 4; ++d) s = dot2(x[d], gw[d], s);
    s += dpp<XOR1>(s); s += dpp<XOR2>(s); s += dpp<ROR4>(s); s += dpp<ROR8>(s);
    s = sw16_add(s, s); s = sw32_add(s, s);
    return __builtin_amdgcn_rcpf(1.f + __builtin_amdgcn_exp2f(-1.4426950408889634f * s));
}
template <int OFF> __device__ __forceinline__ void lds_wait() {
    u32 t, n, c;
    asm volatile("s_mov_b32 %1, 0x100000\n\tv_mov_b32 %0, 0\n"
                 "1:\n\tds_read_b32 %0, %0 offset:%3\n\ts_waitcnt lgkmcnt(0)\n\tv_readfirstlane_b32 %2, %0\n\ts_cmp_lg_u32 %2, 0\n\ts_cbranch_scc1 2f\n\t"
                 "s_sleep 1\n\ts_sub_u32 %1, %1, 1\n\ts_cbranch_scc0 1b\n"
                 "2:" : "=&v"(t), "=&s"(n), "=&s"(c) : "n"(OFF) : "memory", "scc");
}
template <int THR> __device__ __forceinline__ void prog_wait() {
    if (THR <= 0) return;
    u32 t, z, n, c;
    asm volatile("s_mov_b32 %1, 0x200000\n\tv_mov_b32 %3, 0\n"
                 "1:\n\tds_read_b32 %0, %3 offset:56\n\ts_waitcnt lgkmcnt(0)\n\tv_readfirstlane_b32 %2, %0\n\ts_cmp_ge_u32 %2, %4\n\ts_cbranch_scc1 2f\n\t"
                 "s_sub_u32 %1, %1, 1\n\ts_cbranch_scc0 1b\n"
                 "2:" : "=&v"(t), "=&s"(n), "=&s"(c), "=&v"(z) : "n"(THR) : "memory", "scc");
}
template <int N, int V> __device__ __forceinline__ void prog_post() {
    u32 z, v;
    asm volatile("v_mov_b32 %0, 0\n\tv_mov_b32 %1, %3\n\ts_waitcnt vmcnt(%2)\n\tds_max_u32 %0, %1 offset:56" : "=&v"(z), "=&v"(v) : "n"(N), "n"(V) : "memory");
}
constexpr int thr(int p) { return p < PSW ? p + 4 - WW : (p + 4 - WW2 > 20 + GPOS ? p + 4 - WW2 : (p + 4 - WW < 20 + GPOS ? p + 4 - WW : 20 + GPOS)); }
#define BARRIER() do { asm volatile("s_waitcnt lgkmcnt(0)" ::: "memory"); __builtin_amdgcn_s_barrier(); MEMBAR(); } while (0)

__global__ void __launch_bounds__(256, 2) gated_mix_kernel(Args a) {
    __shared__ __attribute__((aligned(16))) char lds_[L_END];
    LAS char* lds = (LAS char*)lds_;
    volatile LAS u32* flag = (volatile LAS u32*)(lds + L_FLAG);
    const int b = blockIdx.x, g = b & 7, s = b >> 3, lane = threadIdx.x & 63;
    int wv = __builtin_amdgcn_readfirstlane(threadIdx.x >> 6);
    ST_DECL; ST(0);
    u64 pc; asm volatile("s_getpc_b64 %0" : "=s"(pc));
    if (threadIdx.x < L_Q / 4) *(volatile LAS u32*)(lds + threadIdx.x * 4) = 0;
    asm volatile("" :: "s"(a.x), "s"(a.k), "s"(a.v), "s"(a.wq), "s"(a.wo), "s"(a.w1), "s"(a.b1), "s"(a.w2), "s"(a.b2), "s"(a.gw), "s"(a.out), "s"(a.ws));
    BARRIER();
    const rsrc_t ws = mk(a.ws, WS_END);
    const u32 kvoff = (u32)(g * SK + (SK - WIN) + s * 128) * 256u;
    u32 cw = __builtin_amdgcn_raw_buffer_load_b32(mk((const char*)(pc & ~255ull), wv == 3 ? CODE_BYTES : 0), (lane & 15) * 128 + (lane >> 4) * 8192, (s & 3) * 2048, 0);
    SB();
    u32 cp = 0;
    if (NTP) {
        const int jj = g * 32 + s;
        u32 pa = OOB;
        if ((NTP & 1) && lane < 16) pa = WS_O + (8 * g + (s >> 2)) * 8192 + ((s & 3) * 16 + lane) * 128;
        if ((NTP & 2) && lane >= 16 && lane < 24) pa = WS_Y + (jj >> 6) * 65536 + ((jj & 7) * 8 + lane - 16) * 1024 + ((jj & 63) >> 3) * 128;
        if ((NTP & 4) && lane == 24) pa = WS_ML + g * 4096 + s * 128;
        if ((NTP & 8) && lane >= 28 && lane < 32) pa = WS_F + g * 16384 + (s * 4 + lane - 28) * 128;
        if ((NTP & 16) && lane >= 32 && lane < 36) pa = WS_Q + g * 16384 + (s * 4 + lane - 32) * 128;
        cp = __builtin_amdgcn_raw_buffer_load_b32(ws, wv == 3 ? pa : OOB, 0, NTP & 32 ? 0 : 2);
        SB();
    }
    const int rho = 8 * g + (s >> 2), cq = s & 3;

    if (wv == 0) {
        const int kq = s & 3, r8 = lane >> 3;
        u32 tag = __builtin_amdgcn_raw_buffer_load_b32(ws, 0, WS_EPOCH, 16);
        u32x4 xr[2], w[16], kf[32];
        const char* xp = a.x + (kq * 128 + r8 * 16) * 2;
        xr[0] = *(const GAS u32x4*)xp; xr[1] = *(const GAS u32x4*)(xp + 16);
        const rsrc_t rq = mk(a.wq + kq * (128 * 8192) + g * 1024 + (s >> 2) * 128, 128 * 8192);
        const u32 wo_ = r8 * (16 * 8192) + (lane & 7) * 16;
#pragma unroll
        for (int i = 0; i < 16; ++i) { w[i] = bnt(rq, wo_, i * 8192); SB(); }
        const int key3 = (lane >> 1) & 7;
        const u32 ko = key3 * 256 + (2 * (lane >> 4) + (lane & 1)) * 16;
        const rsrc_t rk = mk(a.k + kvoff, 32768);
#define KLD(i) bnt(rk, ko + (((i) >> 1) & 1) * 2048 + ((i) & 1) * 128, ((i) >> 2) * 4096)
#pragma unroll
        for (int i = 0; i < WW - 16; ++i) kf[i] = KLD(i);
        SB();
        ST(1);
        const int hd = (lane >> 2) & 3, xb = (lane >> 1) & 1, yb = lane & 1;
        u32 qsa = (lane & 8) ? OOB : ((s >> 2) * 32 + 4 * (lane & 7) + 2 * (lane >> 5) + ((lane >> 4) & 1)) * 16;
        u32 qa0 = L_Q + hd * 320 + (2 * (lane >> 4) + yb) * 16, qa1 = xb ? qa0 : L_Z;
        qa0 = xb ? L_Z : qa0;
        u32 pwa = L_P + hd * 320 + (2 * xb + (lane >> 5)) * 64 + (2 * ((lane >> 4) & 1) + yb) * 16;
        u32 mla = (lane & 0x23) ? OOB : hd * 16 + (lane & 16) * 4;
        u32 l16 = lane * 16;
        PIN(qsa); PIN(qa0); PIN(qa1); PIN(pwa); PIN(mla); PIN(l16);
        SB();
        float acc[8] = {0.f, 0.f, 0.f, 0.f, 0.f, 0.f, 0.f, 0.f};
#pragma unroll
        for (int i = 0; i < 16; ++i) {
            if (WW - 16 + i < 32 && i < 15) { PIN(w[i]); kf[WW - 16 + i] = KLD(WW - 16 + i); SB(); }
            row_fma(acc, w[i], xr[i >> 3][(i >> 1) & 3], i & 1);
            SB();
        }
        float u0, u1;
        reduce8(acc, u0, u1);
        tag += 1;
        ST(2);
        bst(u0, u1, tag, ws, qsa, WS_Q + g * 16384 + kq * 4096);
        flag[10] = tag;
        SB();
        constexpr int NI = WW - 1 < 32 ? WW - 1 : 32;
        if (NI < 32) {
            asm volatile("s_waitcnt vmcnt(%0)" :: "n"(NI + 1 - (32 - NI)) : "memory");
#pragma unroll
            for (int i = NI; i < 32; ++i) kf[i] = KLD(i);
            SB();
        }
#define KP(c) if ((c) <= GPOS) prog_post<32 - (c) + ((c) <= NI ? 1 : 0), 16 + (c)>()
        KP(8); KP(12); KP(16); KP(20); KP(24); KP(28); KP(32);
        ST(3);
        u32x4 qg[16];
#pragma nounroll
        for (u32 n = 0; n < SPIN_CAP; ++n) {
            MEMBAR();
#pragma unroll
            for (int i = 0; i < 16; ++i) qg[i] = bld(ws, l16 + (i >> 2) * 1024, WS_Q + g * 16384 + (i & 3) * 4096);
            SB();
#define KQ(c) if ((c) > GPOS) prog_post<48 - (c) + ((c) <= NI ? 1 : 0), 16 + (c)>()
            KQ(12); KQ(16); KQ(20); KQ(24); KQ(28); KQ(32);
            SB();
            u32 bad = 0;
#pragma unroll
            for (int i = 0; i < 16; ++i) bad = chk(bad, qg[i], tag);
            CNT(8, n); STF(7, n);
            if (__builtin_amdgcn_ballot_w64(bad != 0) == 0) break;
        }
        ST(4);
#pragma unroll
        for (int t = 0; t < 4; ++t) {
            const float q0 = (__uint_as_float(qg[4 * t][0]) + __uint_as_float(qg[4 * t + 1][0])) + (__uint_as_float(qg[4 * t + 2][0]) + __uint_as_float(qg[4 * t + 3][0]));
            const float q1 = (__uint_as_float(qg[4 * t][2]) + __uint_as_float(qg[4 * t + 1][2])) + (__uint_as_float(qg[4 * t + 2][2]) + __uint_as_float(qg[4 * t + 3][2]));
            *(volatile LAS u32*)(lds + L_Q + t * 320 + lane * 4) = cvtpk(q0, q1);
        }
        u32x4 qf[2][2];
        qf[0][0] = *(volatile LAS u32x4*)(lds + qa0); qf[0][1] = *(volatile LAS u32x4*)(lds + qa0 + 128);
        qf[1][0] = *(volatile LAS u32x4*)(lds + qa1); qf[1][1] = *(volatile LAS u32x4*)(lds + qa1 + 128);
        SB();
        float sc[8];
#pragma unroll
        for (int n = 0; n < 8; ++n) {
            f32x4 d = {0.f, 0.f, 0.f, 0.f};
            d = mfma(kf[4 * n], qf[0][0], d); d = mfma(kf[4 * n + 1], qf[0][1], d);
            d = mfma(kf[4 * n + 2], qf[1][0], d); d = mfma(kf[4 * n + 3], qf[1][1], d);
            const float p = yb ? d[3] : d[0], q = yb ? d[1] : d[2];
            sc[n] = p + dpp<XOR1>(q);
        }
        ST(5);
        constexpr float C2 = 0.08838834764831845f * 1.4426950408889634f;
        float m = fmx(fmx(fmx(sc[0], sc[1]), fmx(sc[2], sc[3])), fmx(fmx(sc[4], sc[5]), fmx(sc[6], sc[7])));
        m = fmx(m, dpp<XOR1>(m)); m = fmx(m, dpp<XOR2>(m)); m = sw16_max(m); m = sw32_max(m);
        m *= C2;
        float p[8];
#pragma unroll
        for (int n = 0; n < 8; ++n) p[n] = __builtin_amdgcn_exp2f(fmaf(sc[n], C2, -m));
        *(volatile LAS u32x4*)(lds + pwa) = u32x4{cvtpk(p[0], p[1]), cvtpk(p[2], p[3]), cvtpk(p[4], p[5]), cvtpk(p[6], p[7])};
        flag[5] = 1;
        SB();
        ST(6);
        float l = ((p[0] + p[1]) + (p[2] + p[3])) + ((p[4] + p[5]) + (p[6] + p[7]));
        l += dpp<XOR1>(l); l += dpp<XOR2>(l); l = sw16_add(l, l); l = sw32_add(l, l);
        bst(m, l, tag, ws, mla, WS_ML + g * 4096 + s * 128);
        SB();
        ST(9);
        u32 mga = (lane >> 4) * 1024, wla = L_W + (lane >> 4) * 16;
        lds_wait<52>();
        float gt = __uint_as_float(flag[12]);
        PIN(mga); PIN(wla); PIN(gt);
        __builtin_amdgcn_s_sleep(DELAY_ML);
        u32x4 ml[8];
#pragma nounroll
        for (u32 n = 0; n < SPIN_CAP; ++n) {
            MEMBAR();
#pragma unroll
            for (int k = 0; k < 8; ++k) ml[k] = bld(ws, mga + k * 128, WS_ML + g * 4096 + (s >> 3) * 16);
            u32 bad = 0;
#pragma unroll
            for (int k = 0; k < 8; ++k) bad = chk(bad, ml[k], tag);
            if (__builtin_amdgcn_ballot_w64(bad != 0) == 0) break;
        }
        ST(10);
        float M = fmx(fmx(fmx(__uint_as_float(ml[0][0]), __uint_as_float(ml[1][0])), fmx(__uint_as_float(ml[2][0]), __uint_as_float(ml[3][0]))),
                      fmx(fmx(__uint_as_float(ml[4][0]), __uint_as_float(ml[5][0])), fmx(__uint_as_float(ml[6][0]), __uint_as_float(ml[7][0]))));
        M = sw16_max(M); M = sw32_max(M);
        float wg[8], L = 0.f;
#pragma unroll
        for (int k = 0; k < 8; ++k) { wg[k] = __builtin_amdgcn_exp2f(__uint_as_float(ml[k][0]) - M); L = fmaf(wg[k], __uint_as_float(ml[k][2]), L); }
        L = sw16_add(L, L); L = sw32_add(L, L);
        const float scl = gt * __builtin_amdgcn_rcpf(L);
        *(volatile LAS u32x4*)(lds + wla) = u32x4{cvtpk(wg[0] * scl, wg[1] * scl), cvtpk(wg[2] * scl, wg[3] * scl), cvtpk(wg[4] * scl, wg[5] * scl), cvtpk(wg[6] * scl, wg[7] * scl)};
        flag[9] = 1;
        ST_OUT();
    }
    asm volatile("" : "+s"(wv));
    if (wv == 1) {
        u32 oa = ((lane >> 4) * 2 + ((lane >> 3) & 1)) * 8192 + (lane & 7) * 16;
        u32 pa = L_P + ((lane >> 2) & 3) * 320 + (lane >> 4) * 16;
        u32 l16 = lane * 16;
        PIN(oa); PIN(pa); PIN(l16);
        const rsrc_t rv = mk(a.v + kvoff, 32768);
        if (PREW & 2) { bst(0.f, 0.f, 0, ws, oa, WS_O + g * 65536 + s * 256); bst(0.f, 0.f, 0, ws, oa + 128, WS_O + g * 65536 + s * 256); }
        u32x4 v[32];
#define VLD(c) do { prog_wait<(thr(56 + (c)) <= 56 ? thr(56 + (c)) : 0)>(); if (thr(56 + (c)) > 56) asm volatile("s_waitcnt vmcnt(%0)" :: "n"((c) - (thr(56 + (c)) - 56)) : "memory"); SB(); \
        _Pragma("unroll") for (int i = (c); i < (c) + 4; ++i) v[i] = bnt(rv, l16 + (i >> 3) * 1024, (i & 7) * 4096); SB(); } while (0)
        VLD(0); ST(1); VLD(4); VLD(8); VLD(12); VLD(16); VLD(20); VLD(24); VLD(28);
        flag[4] = 1;
        u32 tag = flag[10]; PIN(tag);
        ST(2);
        u32x4 t0[8], t1[8], t2[8], t3[8], pf[4];
        prog_post<28, 60>(); prog_post<24, 64>();
        transpose8(v, t0);
#pragma unroll
        for (int i = 0; i < 8; ++i) PIN(t0[i]);
        prog_post<20, 68>(); prog_post<16, 72>();
        transpose8(v + 8, t1);
#pragma unroll
        for (int i = 0; i < 8; ++i) PIN(t1[i]);
        prog_post<12, 76>(); prog_post<8, 88>();
        transpose8(v + 16, t2);
#pragma unroll
        for (int i = 0; i < 8; ++i) PIN(t2[i]);
        SB();
#pragma nounroll
        for (u32 n = 0; n < SPIN_CAP * 16; ++n) {
            const u32 f = flag[5];
#pragma unroll
            for (int i = 0; i < 4; ++i) pf[i] = *(volatile LAS u32x4*)(lds + pa + i * 64);
            if (__builtin_amdgcn_readfirstlane(f)) break;
        }
        ST(3);
        f32x4 o[8];
#pragma unroll
        for (int e = 0; e < 8; ++e) { o[e] = mfma(pf[0], t0[e], f32x4{0.f, 0.f, 0.f, 0.f}); }
#pragma unroll
        for (int e = 0; e < 8; ++e) o[e] = mfma(pf[1], t1[e], o[e]);
#pragma unroll
        for (int e = 0; e < 8; ++e) o[e] = mfma(pf[2], t2[e], o[e]);
        transpose8(v + 24, t3);
        ST(4);
#pragma unroll
        for (int e = 0; e < 8; ++e) o[e] = mfma(pf[3], t3[e], o[e]);
#pragma unroll
        for (int k = 0; k < 2; ++k) bstu(cvtpk(o[4 * k][0], o[4 * k + 1][0]), cvtpk(o[4 * k + 2][0], o[4 * k + 3][0]), tag, ws, oa + k * 128, WS_O + g * 65536 + s * 256);
        SB();
        ST(10);
        __builtin_amdgcn_s_sleep(DELAY_O);
        flag[6] = 1;
        ST_OUT();
    }
    asm volatile("" : "+s"(wv));
    if (wv == 2) {
        const int kgrp = s >> 2, r8 = lane >> 3;
        const int c0 = 224 * g - 32 * (g & 1) + 64 * (s & 3);
        const int j = g * 32 + s, col = 128 * (j >> 6) + 8 * (j & 15) + 2 * ((j >> 4) & 3);
        const u32x4 xa = *(const GAS u32x4*)(a.x + lane * 16), gw = *(const GAS u32x4*)(a.gw + lane * 16);
        const u32x4 xr = *(const GAS u32x4*)(a.x + (kgrp * 64 + r8 * 8) * 2);
        const int pr = (lane & 15) < 14 ? (lane & 15) : 13;
        const u32 b1w = *(const GAS u32*)(a.b1 + (rho * 14 + pr) * 4);
        const u32 b2w = __builtin_amdgcn_raw_buffer_load_b32(mk(a.b2, 1024), 0, col * 2, 0);
        SB();
        const rsrc_t r1 = mk(a.w1 + kgrp * (64 * 3584) + c0 * 2, 64 * 3584 - c0 * 2);
        const int cl = c0 + 8 * (lane & 7) - 224 * g;
        u32 wa = (cl < 0 || cl >= 224) ? OOB : r8 * (8 * 3584) + (lane & 7) * 16;
        u32 fsa = (lane & 8) ? OOB : ((s & 3) * 32 + 4 * (lane & 7) + 2 * (lane >> 5) + ((lane >> 4) & 1)) * 16;
        u32 fa = (lane >> 4) * 2048 + ((s >> 2) * 14 + 16 * (g & 1) + pr) * 16;
        u32 ha = lane < 14 ? L_A + lane * 4 : L_DUMMY;
        u32 ya = lane * 1024, outa = (lane & 31) ? OOB : (lane >> 4), epa = (b | lane) ? OOB : 0;
        PIN(wa); PIN(fsa); PIN(fa); PIN(ha); PIN(ya); PIN(outa); PIN(epa);
        u32x4 w[8];
        prog_wait<(52 - WW > 24 ? 52 - WW : 24)>();
#pragma unroll
        for (int i = 0; i < 4; ++i) w[i] = bnt(r1, wa, i * 3584);
        SB();
        prog_wait<56 - WW>();
#pragma unroll
        for (int i = 4; i < 8; ++i) w[i] = bnt(r1, wa, i * 3584);
        SB();
        ST(2);
        const float gt = gate_of(xa, gw);
        flag[12] = __float_as_uint(gt);
        flag[13] = 1;
        float g1 = 1.f - gt, bb0 = bf_lo(b1w), bb1 = bf_hi(b1w);
        float bias = g1 * (lane < 32 ? bf_lo(b2w) : bf_hi(b2w));
        u32 tag = flag[10];
        PIN(g1); PIN(bb0); PIN(bb1); PIN(bias); PIN(tag);
        SB();
        float acc[8] = {0.f, 0.f, 0.f, 0.f, 0.f, 0.f, 0.f, 0.f};
#pragma unroll
        for (int i = 0; i < 8; ++i) {
            if (i == 3) prog_post<4, 52>();
            if (i == 7) prog_post<0, 56>();
            row_fma(acc, w[i], xr[i >> 1], i & 1);
            SB();
        }
        float u0, u1;
        reduce8(acc, u0, u1);
        bst(u0, u1, tag, ws, fsa, WS_F + g * 16384 + kgrp * 2048);
        SB();
        ST(3);
        lds_wait<F_TRIG * 4>();
        __builtin_amdgcn_s_sleep(DELAY_F);
        u32x4 f0 = bld(ws, fa, WS_F + g * 16384), f1 = bld(ws, fa, WS_F + g * 16384 + 8192);
        SB();
        ST(10);
        if (__builtin_expect(__builtin_amdgcn_ballot_w64(chk(chk(0, f0, tag), f1, tag) != 0) != 0, 0)) {
#pragma nounroll
            for (u32 n = 1; n < SPIN_CAP; ++n) {
                MEMBAR();
                f0 = bld(ws, fa, WS_F + g * 16384); f1 = bld(ws, fa, WS_F + g * 16384 + 8192);
                CNT(8, n);
                if (__builtin_amdgcn_ballot_w64(chk(chk(0, f0, tag), f1, tag) != 0) == 0) break;
            }
        }
        ST(4);
        float h0 = __uint_as_float(f0[0]) + __uint_as_float(f1[0]), h1 = __uint_as_float(f0[2]) + __uint_as_float(f1[2]);
        h0 = sw16_add(h0, h0); h1 = sw16_add(h1, h1);
        h0 = sw32_add(h0, h0) + bb0; h1 = sw32_add(h1, h1) + bb1;
        constexpr float GC = -2.f * 0.7978845608028654f * 1.4426950408889634f;
        h0 = h0 * __builtin_amdgcn_rcpf(1.f + __builtin_amdgcn_exp2f(GC * fmaf(0.044715f * h0 * h0, h0, h0)));
        h1 = h1 * __builtin_amdgcn_rcpf(1.f + __builtin_amdgcn_exp2f(GC * fmaf(0.044715f * h1 * h1, h1, h1)));
        *(volatile LAS u32*)(lds + ha) = cvtpk(h0 * g1, h1 * g1);
        flag[7] = 1;
        SB();
        ST(5);
        u32x4 y;
        if (PRIME & 2) { y = bld(ws, ya, WS_Y + (j >> 6) * 65536 + (j & 63) * 16); PIN(y); }
        lds_wait<32>();
#pragma nounroll
        for (u32 n = 0; n < SPIN_CAP; ++n) {
            MEMBAR();
            const u32x4 yn = bld(ws, ya, WS_Y + (j >> 6) * 65536 + (j & 63) * 16);
            const bool ok = ya != OOB && chk(0, yn, tag) == 0;
            if (ok) { y = yn; ya = OOB; }
            const u32 bad = ya != OOB;
            CNT(9, n); STF(7, n);
            if (__builtin_amdgcn_ballot_w64(bad != 0) == 0) break;
        }
        ST(6);
        float r = sw32_add(__uint_as_float(y[0]), __uint_as_float(y[2]));
        r = sw16_add(r, r); r += dpp<ROR8>(r); r += dpp<ROR4>(r); r += dpp<XOR2>(r); r += dpp<XOR1>(r);
        r += bias;
        __builtin_amdgcn_raw_buffer_store_b16((unsigned short)cvtpk(r, r), mk(a.out + col * 2, 4), outa, 0, 0);
        __builtin_amdgcn_raw_buffer_store_b32(tag, ws, epa, WS_EPOCH, 16);
        ST_OUT();
    }
    asm volatile("" : "+s"(wv));
    if (wv == 3) {
        const int kg = lane >> 4;
        const rsrc_t r2 = mk(a.w2 + rho * (28 * 1024) + cq * 256, 28 * 1024 - cq * 256);
        const rsrc_t ro = mk(a.wo + rho * (64 * 1024) + cq * 256, 64 * 1024 - cq * 256);
        u32 w2a = kg * 8192 + (lane & 15) * 16, w2b = kg == 3 ? w2a - 4096 : w2a;
        u32 woa = (32 * (kg & 1) + 4 * (kg >> 1)) * 1024 + (lane & 15) * 16;
        u32 oga = kg * 2048 + (lane & 15) * 16, l16 = lane * 16, aa = L_A + kg * 16;
        PIN(w2a); PIN(w2b); PIN(woa); PIN(oga); PIN(l16); PIN(aa);
        if (PREW & 8) bst(0.f, 0.f, 0, ws, l16, WS_Y + cq * 65536 + rho * 1024);
        u32x4 r[8], u[16], t2[8], ta[8], tb[8];
        lds_wait<16>();
#pragma unroll
        for (int c = 0; c < 8; c += 4) {
            if (c == 0) prog_wait<thr(88)>(); else prog_wait<thr(92)>();
#pragma unroll
            for (int i = c; i < c + 4; ++i) r[i] = bnt(r2, (i < 4 ? w2a : w2b) + (i & 3) * 1024, (i >> 2) * 4096);
            SB();
        }
        ST(1);
#define ULD(c) do { prog_wait<(thr(96 + (c)) < 80 ? thr(96 + (c)) : 88)>(); _Pragma("unroll") for (int i = (c); i < (c) + 4; ++i) u[i] = bnt(ro, woa + (2 * (i >> 3) + ((i >> 2) & 1)) * 1024, (i & 3) * 8192); SB(); } while (0)
        ULD(0); ULD(4); ULD(8); ULD(12);
        ST(2);
        u32 tag = flag[10];
        PIN(tag); PIN(cw); PIN(cp);
        transpose8(r, t2);
#pragma unroll
        for (int i = 0; i < 8; ++i) PIN(t2[i]);
        transpose8(u, ta);
        transpose8(u + 8, tb);
#pragma unroll
        for (int i = 0; i < 8; ++i) { PIN(ta[i]); PIN(tb[i]); }
        if (PREY) bst(0.f, 0.f, tag - 1, ws, l16, WS_Y + cq * 65536 + rho * 1024);
        ST(5);
        lds_wait<24>();
        if (GATE_W) lds_wait<36>();
        ST(4);
        u32x4 og[8];
#pragma unroll
        for (int k = 0; k < 8; ++k) og[k] = bld(ws, oga + k * 256, WS_O + rho * 8192);
        SB();
        lds_wait<28>();
        const u32x4 af = *(volatile LAS u32x4*)(lds + aa);
        f32x4 d[8];
#pragma unroll
        for (int e = 0; e < 8; ++e) { d[e] = mfma(af, t2[e], f32x4{0.f, 0.f, 0.f, 0.f}); PIN(d[e]); }
        if (!GATE_W) lds_wait<36>();
        u32x4 wf = *(volatile LAS u32x4*)(lds + aa + (L_W - L_A));
        PIN(wf);
        SB();
        ST(3);
        {
            u32 bad = 0;
#pragma unroll
            for (int k = 0; k < 8; ++k) bad = chk(bad, og[k], tag);
            ST(10);
            if (__builtin_expect(__builtin_amdgcn_ballot_w64(bad != 0) != 0, 0)) {
#pragma nounroll
                for (u32 n = 1; n < SPIN_CAP; ++n) {
                    MEMBAR();
#pragma unroll
                    for (int k = 0; k < 8; ++k) og[k] = bld(ws, oga + k * 256, WS_O + rho * 8192);
                    bad = 0;
#pragma unroll
                    for (int k = 0; k < 8; ++k) bad = chk(bad, og[k], tag);
                    CNT(9, n);
                    if (__builtin_amdgcn_ballot_w64(bad != 0) == 0) break;
                }
            }
        }
        ST(7);
        f32x4 c[4];
#pragma unroll
        for (int e = 0; e < 4; ++e) {
            u32x4 ot;
#pragma unroll
            for (int p = 0; p < 4; ++p) ot[p] = __builtin_amdgcn_perm(og[2 * p + 1][e & 2], og[2 * p][e & 2], (e & 1) ? 0x07060302u : 0x05040100u);
            c[e] = mfma(ot, wf, f32x4{0.f, 0.f, 0.f, 0.f});
        }
        const u32x4 a0 = {cvtpk(c[0][0], c[0][1]), cvtpk(c[0][2], c[0][3]), cvtpk(c[1][0], c[1][1]), cvtpk(c[1][2], c[1][3])};
        const u32x4 a1 = {cvtpk(c[2][0], c[2][1]), cvtpk(c[2][2], c[2][3]), cvtpk(c[3][0], c[3][1]), cvtpk(c[3][2], c[3][3])};
#pragma unroll
        for (int e = 0; e < 8; ++e) d[e] = mfma(a0, ta[e], d[e]);
#pragma unroll
        for (int e = 0; e < 8; ++e) d[e] = mfma(a1, tb[e], d[e]);
        const bool k0 = kg & 1, k1 = kg & 2;
        const float ya0 = k0 ? d[2][0] : d[0][0], yb0 = k0 ? d[6][0] : d[4][0], ya1 = k0 ? d[3][0] : d[1][0], yb1 = k0 ? d[7][0] : d[5][0];
        const float y0 = k1 ? yb0 : ya0, y1 = k1 ? yb1 : ya1;
        bst(y0, y1, tag, ws, l16, WS_Y + cq * 65536 + rho * 1024);
        SB();
        ST(6);
        __builtin_amdgcn_s_sleep(DELAY_Y);
        flag[8] = 1;
        ST_OUT();
    }
}

extern "C" void kernel_launch(void* const* d_in, const int* in_sizes, int n_in, void* d_out, int out_size, void* d_ws, size_t ws_size, hipStream_t stream) {
    Args a;
    a.x = (const char*)d_in[0]; a.k = (const char*)d_in[1]; a.v = (const char*)d_in[2]; a.wq = (const char*)d_in[3]; a.wo = (const char*)d_in[4];
    a.w1 = (const char*)d_in[6]; a.b1 = (const char*)d_in[7]; a.w2 = (const char*)d_in[8]; a.b2 = (const char*)d_in[9]; a.gw = (const char*)d_in[10];
    a.out = (char*)d_out; a.ws = (char*)d_ws + (WSOFF ? ws_size - (8u << 20) : 0);
    gated_mix_kernel<<<256, 256, 0, stream>>>(a);
}
```

```cpp
#include <hip/hip_runtime.h>
#include <stdint.h>

typedef unsigned u32;
typedef unsigned long long u64;
typedef u32 u32x4 __attribute__((ext_vector_type(4)));
typedef float f32x4 __attribute__((ext_vector_type(4)));
typedef __bf16 bf16x8 __attribute__((ext_vector_type(8)));
typedef __bf16 bf16x2 __attribute__((ext_vector_type(2)));
#define GAS __attribute__((address_space(1)))
#define LAS __attribute__((address_space(3)))
#define SB() __builtin_amdgcn_sched_barrier(0)
#define MEMBAR() asm volatile("" ::: "memory")

#define GATHER_AT 1
#define VHOLD 16
#define DELAY_ML 20
#define DELAY_O 16
#define DELAY_Y 16
#define WO_FIRST 0
#define DELAY_F 8
#define F_TRIG 5
#define K0 32
#define REL_F 29
#define REL_T 9
#define VWIN 32
#define PRIME 0
#define REL_V 13
#define PREY 0
#define W2_LATE 1
#define GATE_W 1
#define WW 28
#define GPOS 28
#define NTP 0
#define WW2 WW
#define PSW 72
#define PRET 0
#define PREW 0
#define WSOFF 0

constexpr int SK = 65536, WIN = 4096;
constexpr u32 SPIN_CAP = 1u << 16;
constexpr u32 WS_EPOCH = 0;
constexpr u32 WS_Q = 4096;
constexpr u32 WS_ML = WS_Q + 8 * 16384;
constexpr u32 WS_F = WS_ML + 32768;
constexpr u32 WS_O = WS_F + 64 * 2048;
constexpr u32 WS_Y = WS_O + (1u << 19);
constexpr u32 WS_END = WS_Y + (1u << 18);
constexpr u32 WS_STAMP = 4u << 20;
constexpr int L_FLAG = 0;
constexpr int L_DUMMY = 60;
constexpr int L_A = 64;
constexpr int L_W = 384;
constexpr int L_Z = 128;
constexpr int L_Q = 512;
constexpr int L_P = L_Q + 1280;
constexpr int L_END = L_P + 1280;

struct Args {
    const char* x; const char* k; const char* v; const char* wq; const char* wo;
    const char* w1; const char* b1; const char* w2; const char* b2; const char* gw;
    char* out; char* ws;
};

#define ST(i)
#define CNT(i, n)
#define STF(i, n)
#define ST_DECL
#define ST_OUT()

__device__ __forceinline__ u32x4 ldnt(const char* p) { return __builtin_nontemporal_load((const GAS u32x4*)p); }
typedef __amdgpu_buffer_rsrc_t rsrc_t;
__device__ __forceinline__ rsrc_t mk(const char* p, u32 n) { return __builtin_amdgcn_make_buffer_rsrc((void*)p, 0, n, 0x00020000); }
__device__ __forceinline__ u32x4 bnt(rsrc_t r, u32 v, u32 s) { return __builtin_amdgcn_raw_buffer_load_b128(r, v, s, 2); }
__device__ __forceinline__ u32x4 bld(rsrc_t r, u32 v, u32 s) { return __builtin_amdgcn_raw_buffer_load_b128(r, v, s, 16); }
__device__ __forceinline__ void bstu(u32 a, u32 b, u32 tag, rsrc_t r, u32 v, u32 s) { __builtin_amdgcn_raw_buffer_store_b128(u32x4{a, tag, b, tag}, r, v, s, 16); }
__device__ __forceinline__ u32 chk(u32 bad, u32x4 x, u32 tag) { bad |= x[1] ^ tag; asm volatile("" : "+v"(bad)); bad |= x[3] ^ tag; asm volatile("" : "+v"(bad)); return bad; }
__device__ __forceinline__ void bst(float a, float b, u32 tag, rsrc_t r, u32 v, u32 s) { __builtin_amdgcn_raw_buffer_store_b128(u32x4{__float_as_uint(a), tag, __float_as_uint(b), tag}, r, v, s, 16); }
#define PIN(x) asm volatile("" : "+v"(x))
constexpr u32 OOB = 0x80000000u;
constexpr u32 CODE_BYTES = 12800;
__device__ __forceinline__ float dot2(u32 a, u32 b, float c) { return __builtin_amdgcn_fdot2_f32_bf16(__builtin_bit_cast(bf16x2, a), __builtin_bit_cast(bf16x2, b), c, false); }
__device__ __forceinline__ u32 cvtpk(float lo, float hi) { bf16x2 r = {(__bf16)lo, (__bf16)hi}; return __builtin_bit_cast(u32, r); }
template <int CTRL> __device__ __forceinline__ float dpp(float x) { return __builtin_bit_cast(float, __builtin_amdgcn_mov_dpp(__builtin_bit_cast(int, x), CTRL, 0xf, 0xf, true)); }
constexpr int XOR1 = 0xB1, XOR2 = 0x4E, ROR4 = 0x124, ROR8 = 0x128;
__device__ __forceinline__ float sw16_add(float a, float b) { auto s = __builtin_amdgcn_permlane16_swap(__float_as_uint(a), __float_as_uint(b), false, false); return __uint_as_float(s[0]) + __uint_as_float(s[1]); }
__device__ __forceinline__ float sw32_add(float a, float b) { auto s = __builtin_amdgcn_permlane32_swap(__float_as_uint(a), __float_as_uint(b), false, false); return __uint_as_float(s[0]) + __uint_as_float(s[1]); }
__device__ __forceinline__ float fmx(float a, float b) { return __builtin_elementwise_maximum(a, b); }
__device__ __forceinline__ float sw16_max(float a) { auto s = __builtin_amdgcn_permlane16_swap(__float_as_uint(a), __float_as_uint(a), false, false); return fmx(__uint_as_float(s[0]), __uint_as_float(s[1])); }
__device__ __forceinline__ float sw32_max(float a) { auto s = __builtin_amdgcn_permlane32_swap(__float_as_uint(a), __float_as_uint(a), false, false); return fmx(__uint_as_float(s[0]), __uint_as_float(s[1])); }
__device__ __forceinline__ f32x4 mfma(u32x4 a, u32x4 b, f32x4 c) { return __builtin_amdgcn_mfma_f32_16x16x32_bf16(__builtin_bit_cast(bf16x8, a), __builtin_bit_cast(bf16x8, b), c, 0, 0, 0); }
__device__ __forceinline__ float bf_lo(u32 w) { return __uint_as_float(w << 16); }
__device__ __forceinline__ float bf_hi(u32 w) { return __uint_as_float(w & 0xffff0000u); }

__device__ __forceinline__ void transpose8(const u32x4* r, u32x4* t) {
#pragma unroll
    for (int p = 0; p < 4; ++p)
#pragma unroll
        for (int d = 0; d < 4; ++d) {
            t[2 * d][p] = __builtin_amdgcn_perm(r[2 * p + 1][d], r[2 * p][d], 0x05040100u);
            t[2 * d + 1][p] = __builtin_amdgcn_perm(r[2 * p + 1][d], r[2 * p][d], 0x07060302u);
        }
}
__device__ __forceinline__ void row_fma(float* acc, u32x4 w, u32 xpair, int odd) {
    const u32 xl = odd ? xpair >> 16 : xpair & 0xffffu, xh = odd ? xpair & 0xffff0000u : xpair << 16;
#pragma unroll
    for (int d = 0; d < 4; ++d) { acc[2 * d] = dot2(w[d], xl, acc[2 * d]); acc[2 * d + 1] = dot2(w[d], xh, acc[2 * d + 1]); }
}
__device__ __forceinline__ void reduce8(const float* acc, float& u0, float& u1) {
    const float s0 = sw32_add(acc[0], acc[4]), s1 = sw32_add(acc[1], acc[5]), s2 = sw32_add(acc[2], acc[6]), s3 = sw32_add(acc[3], acc[7]);
    u0 = sw16_add(s0, s2); u1 = sw16_add(s1, s3);
    u0 += dpp<ROR8>(u0); u1 += dpp<ROR8>(u1);
}
__device__ __forceinline__ float gate_of(u32x4 x, u32x4 gw) {
    float s = 0.f;
#pragma unroll
    for (int d = 0; d < 4; ++d) s = dot2(x[d], gw[d], s);
    s += dpp<XOR1>(s); s += dpp<XOR2>(s); s += dpp<ROR4>(s); s += dpp<ROR8>(s);
    s = sw16_add(s, s); s = sw32_add(s, s);
    return __builtin_amdgcn_rcpf(1.f + __builtin_amdgcn_exp2f(-1.4426950408889634f * s));
}
template <int OFF> __device__ __forceinline__ void lds_wait() {
    u32 t, n, c;
    asm volatile("s_mov_b32 %1, 0x100000\n\tv_mov_b32 %0, 0\n"
                 "1:\n\tds_read_b32 %0, %0 offset:%3\n\ts_waitcnt lgkmcnt(0)\n\tv_readfirstlane_b32 %2, %0\n\ts_cmp_lg_u32 %2, 0\n\ts_cbranch_scc1 2f\n\t"
                 "s_sleep 1\n\ts_sub_u32 %1, %1, 1\n\ts_cbranch_scc0 1b\n"
                 "2:" : "=&v"(t), "=&s"(n), "=&s"(c) : "n"(OFF) : "memory", "scc");
}
template <int THR> __device__ __forceinline__ void prog_wait() {
    if (THR <= 0) return;
    u32 t, z, n, c;
    asm volatile("s_mov_b32 %1, 0x200000\n\tv_mov_b32 %3, 0\n"
                 "1:\n\tds_read_b32 %0, %3 offset:56\n\ts_waitcnt lgkmcnt(0)\n\tv_readfirstlane_b32 %2, %0\n\ts_cmp_ge_u32 %2, %4\n\ts_cbranch_scc1 2f\n\t"
                 "s_sub_u32 %1, %1, 1\n\ts_cbranch_scc0 1b\n"
                 "2:" : "=&v"(t), "=&s"(n), "=&s"(c), "=&v"(z) : "n"(THR) : "memory", "scc");
}
template <int N, int V> __device__ __forceinline__ void prog_post() {
    u32 z, v;
    asm volatile("v_mov_b32 %0, 0\n\tv_mov_b32 %1, %3\n\ts_waitcnt vmcnt(%2)\n\tds_max_u32 %0, %1 offset:56" : "=&v"(z), "=&v"(v) : "n"(N), "n"(V) : "memory");
}
constexpr int thr(int p) { return p < PSW ? p + 4 - WW : (p + 4 - WW2 > 20 + GPOS ? p + 4 - WW2 : (p + 4 - WW < 20 + GPOS ? p + 4 - WW : 20 + GPOS)); }
#define BARRIER() do { asm volatile("s_waitcnt lgkmcnt(0)" ::: "memory"); __builtin_amdgcn_s_barrier(); MEMBAR(); } while (0)

__global__ void __launch_bounds__(256, 2) gated_mix_kernel(const char* p_wq, const char* p_x, const char* p_k, char* p_ws, const char* p_v, const char* p_w1, const char* p_gw,
                                                           const char* p_wo, const char* p_b1, const char* p_w2, const char* p_b2, char* p_out) {
    Args a; a.x = p_x; a.k = p_k; a.v = p_v; a.wq = p_wq; a.wo = p_wo; a.w1 = p_w1; a.b1 = p_b1; a.w2 = p_w2; a.b2 = p_b2; a.gw = p_gw; a.out = p_out; a.ws = p_ws;
    __shared__ __attribute__((aligned(16))) char lds_[L_END];
    LAS char* lds = (LAS char*)lds_;
    volatile LAS u32* flag = (volatile LAS u32*)(lds + L_FLAG);
    const int b = blockIdx.x, g = b & 7, s = b >> 3, lane = threadIdx.x & 63;
    int wv = __builtin_amdgcn_readfirstlane(threadIdx.x >> 6);
    ST_DECL; ST(0);
    u64 pc; asm volatile("s_getpc_b64 %0" : "=s"(pc));
    if (threadIdx.x < L_Q / 4) *(volatile LAS u32*)(lds + threadIdx.x * 4) = 0;
    asm volatile("" :: "s"(a.x), "s"(a.k), "s"(a.v), "s"(a.wq), "s"(a.wo), "s"(a.w1), "s"(a.b1), "s"(a.w2), "s"(a.b2), "s"(a.gw), "s"(a.out), "s"(a.ws));
    BARRIER();
    const rsrc_t ws = mk(a.ws, WS_END);
    const u32 kvoff = (u32)(g * SK + (SK - WIN) + s * 128) * 256u;
    u32 cw = __builtin_amdgcn_raw_buffer_load_b32(mk((const char*)(pc & ~255ull), wv == 3 ? CODE_BYTES : 0), (lane & 15) * 128 + (lane >> 4) * 8192, (s & 3) * 2048, 0);
    SB();
    u32 cp = 0;
    if (NTP) {
        const int jj = g * 32 + s;
        u32 pa = OOB;
        if ((NTP & 1) && lane < 16) pa = WS_O + (8 * g + (s >> 2)) * 8192 + ((s & 3) * 16 + lane) * 128;
        if ((NTP & 2) && lane >= 16 && lane < 24) pa = WS_Y + (jj >> 6) * 65536 + ((jj & 7) * 8 + lane - 16) * 1024 + ((jj & 63) >> 3) * 128;
        if ((NTP & 4) && lane == 24) pa = WS_ML + g * 4096 + s * 128;
        if ((NTP & 8) && lane >= 28 && lane < 32) pa = WS_F + g * 16384 + (s * 4 + lane - 28) * 128;
        if ((NTP & 16) && lane >= 32 && lane < 36) pa = WS_Q + g * 16384 + (s * 4 + lane - 32) * 128;
        cp = __builtin_amdgcn_raw_buffer_load_b32(ws, wv == 3 ? pa : OOB, 0, NTP & 32 ? 0 : 2);
        SB();
    }
    const int rho = 8 * g + (s >> 2), cq = s & 3;

    if (wv == 0) {
        const int kq = s & 3, r8 = lane >> 3;
        u32 tag = __builtin_amdgcn_raw_buffer_load_b32(ws, 0, WS_EPOCH, 16);
        u32x4 xr[2], w[16], kf[32];
        const char* xp = a.x + (kq * 128 + r8 * 16) * 2;
        xr[0] = *(const GAS u32x4*)xp; xr[1] = *(const GAS u32x4*)(xp + 16);
        const rsrc_t rq = mk(a.wq + kq * (128 * 8192) + g * 1024 + (s >> 2) * 128, 128 * 8192);
        const u32 wo_ = r8 * (16 * 8192) + (lane & 7) * 16;
#pragma unroll
        for (int i = 0; i < 16; ++i) { w[i] = bnt(rq, wo_, i * 8192); SB(); }
        const int key3 = (lane >> 1) & 7;
        const u32 ko = key3 * 256 + (2 * (lane >> 4) + (lane & 1)) * 16;
        const rsrc_t rk = mk(a.k + kvoff, 32768);
#define KLD(i) bnt(rk, ko + (((i) >> 1) & 1) * 2048 + ((i) & 1) * 128, ((i) >> 2) * 4096)
#pragma unroll
        for (int i = 0; i < WW - 16; ++i) kf[i] = KLD(i);
        SB();
        ST(1);
        const int hd = (lane >> 2) & 3, xb = (lane >> 1) & 1, yb = lane & 1;
        u32 qsa = (lane & 8) ? OOB : ((s >> 2) * 32 + 4 * (lane & 7) + 2 * (lane >> 5) + ((lane >> 4) & 1)) * 16;
        u32 qa0 = L_Q + hd * 320 + (2 * (lane >> 4) + yb) * 16, qa1 = xb ? qa0 : L_Z;
        qa0 = xb ? L_Z : qa0;
        u32 pwa = L_P + hd * 320 + (2 * xb + (lane >> 5)) * 64 + (2 * ((lane >> 4) & 1) + yb) * 16;
        u32 mla = (lane & 0x23) ? OOB : hd * 16 + (lane & 16) * 4;
        u32 l16 = lane * 16;
        PIN(qsa); PIN(qa0); PIN(qa1); PIN(pwa); PIN(mla); PIN(l16);
        SB();
        float acc[8] = {0.f, 0.f, 0.f, 0.f, 0.f, 0.f, 0.f, 0.f};
#pragma unroll
        for (int i = 0; i < 16; ++i) {
            if (WW - 16 + i < 32 && i < 15) { PIN(w[i]); kf[WW - 16 + i] = KLD(WW - 16 + i); SB(); }
            row_fma(acc, w[i], xr[i >> 3][(i >> 1) & 3], i & 1);
            SB();
        }
        float u0, u1;
        reduce8(acc, u0, u1);
        tag += 1;
        ST(2);
        bst(u0, u1, tag, ws, qsa, WS_Q + g * 16384 + kq * 4096);
        flag[10] = tag;
        SB();
        constexpr int NI = WW - 1 < 32 ? WW - 1 : 32;
        if (NI < 32) {
            asm volatile("s_waitcnt vmcnt(%0)" :: "n"(NI + 1 - (32 - NI)) : "memory");
#pragma unroll
            for (int i = NI; i < 32; ++i) kf[i] = KLD(i);
            SB();
        }
#define KP(c) if ((c) <= GPOS) prog_post<32 - (c) + ((c) <= NI ? 1 : 0), 16 + (c)>()
        KP(8); KP(12); KP(16); KP(20); KP(24); KP(28); KP(32);
        ST(3);
        u32x4 qg[16];
#pragma nounroll
        for (u32 n = 0; n < SPIN_CAP; ++n) {
            MEMBAR();
#pragma unroll
            for (int i = 0; i < 16; ++i) qg[i] = bld(ws, l16 + (i >> 2) * 1024, WS_Q + g * 16384 + (i & 3) * 4096);
            SB();
#define KQ(c) if ((c) > GPOS) prog_post<48 - (c) + ((c) <= NI ? 1 : 0), 16 + (c)>()
            KQ(12); KQ(16); KQ(20); KQ(24); KQ(28); KQ(32);
            SB();
            u32 bad = 0;
#pragma unroll
            for (int i = 0; i < 16; ++i) bad = chk(bad, qg[i], tag);
            CNT(8, n); STF(7, n);
            if (__builtin_amdgcn_ballot_w64(bad != 0) == 0) break;
        }
        ST(4);
#pragma unroll
        for (int t = 0; t < 4; ++t) {
            const float q0 = (__uint_as_float(qg[4 * t][0]) + __uint_as_float(qg[4 * t + 1][0])) + (__uint_as_float(qg[4 * t + 2][0]) + __uint_as_float(qg[4 * t + 3][0]));
            const float q1 = (__uint_as_float(qg[4 * t][2]) + __uint_as_float(qg[4 * t + 1][2])) + (__uint_as_float(qg[4 * t + 2][2]) + __uint_as_float(qg[4 * t + 3][2]));
            *(volatile LAS u32*)(lds + L_Q + t * 320 + lane * 4) = cvtpk(q0, q1);
        }
        u32x4 qf[2][2];
        qf[0][0] = *(volatile LAS u32x4*)(lds + qa0); qf[0][1] = *(volatile LAS u32x4*)(lds + qa0 + 128);
        qf[1][0] = *(volatile LAS u32x4*)(lds + qa1); qf[1][1] = *(volatile LAS u32x4*)(lds + qa1 + 128);
        SB();
        float sc[8];
#pragma unroll
        for (int n = 0; n < 8; ++n) {
            f32x4 d = {0.f, 0.f, 0.f, 0.f};
            d = mfma(kf[4 * n], qf[0][0], d); d = mfma(kf[4 * n + 1], qf[0][1], d);
            d = mfma(kf[4 * n + 2], qf[1][0], d); d = mfma(kf[4 * n + 3], qf[1][1], d);
            const float p = yb ? d[3] : d[0], q = yb ? d[1] : d[2];
            sc[n] = p + dpp<XOR1>(q);
        }
        ST(5);
        constexpr float C2 = 0.08838834764831845f * 1.4426950408889634f;
        float m = fmx(fmx(fmx(sc[0], sc[1]), fmx(sc[2], sc[3])), fmx(fmx(sc[4], sc[5]), fmx(sc[6], sc[7])));
        m = fmx(m, dpp<XOR1>(m)); m = fmx(m, dpp<XOR2>(m)); m = sw16_max(m); m = sw32_max(m);
        m *= C2;
        float p[8];
#pragma unroll
        for (int n = 0; n < 8; ++n) p[n] = __builtin_amdgcn_exp2f(fmaf(sc[n], C2, -m));
        *(volatile LAS u32x4*)(lds + pwa) = u32x4{cvtpk(p[0], p[1]), cvtpk(p[2], p[3]), cvtpk(p[4], p[5]), cvtpk(p[6], p[7])};
        flag[5] = 1;
        SB();
        ST(6);
        float l = ((p[0] + p[1]) + (p[2] + p[3])) + ((p[4] + p[5]) + (p[6] + p[7]));
        l += dpp<XOR1>(l); l += dpp<XOR2>(l); l = sw16_add(l, l); l = sw32_add(l, l);
        bst(m, l, tag, ws, mla, WS_ML + g * 4096 + s * 128);
        SB();
        ST(9);
        u32 mga = (lane >> 4) * 1024, wla = L_W + (lane >> 4) * 16;
        lds_wait<52>();
        float gt = __uint_as_float(flag[12]);
        PIN(mga); PIN(wla); PIN(gt);
        __builtin_amdgcn_s_sleep(DELAY_ML);
        u32x4 ml[8];
#pragma nounroll
        for (u32 n = 0; n < SPIN_CAP; ++n) {
            MEMBAR();
#pragma unroll
            for (int k = 0; k < 8; ++k) ml[k] = bld(ws, mga + k * 128, WS_ML + g * 4096 + (s >> 3) * 16);
            u32 bad = 0;
#pragma unroll
            for (int k = 0; k < 8; ++k) bad = chk(bad, ml[k], tag);
            if (__builtin_amdgcn_ballot_w64(bad != 0) == 0) break;
        }
        ST(10);
        float M = fmx(fmx(fmx(__uint_as_float(ml[0][0]), __uint_as_float(ml[1][0])), fmx(__uint_as_float(ml[2][0]), __uint_as_float(ml[3][0]))),
                      fmx(fmx(__uint_as_float(ml[4][0]), __uint_as_float(ml[5][0])), fmx(__uint_as_float(ml[6][0]), __uint_as_float(ml[7][0]))));
        M = sw16_max(M); M = sw32_max(M);
        float wg[8], L = 0.f;
#pragma unroll
        for (int k = 0; k < 8; ++k) { wg[k] = __builtin_amdgcn_exp2f(__uint_as_float(ml[k][0]) - M); L = fmaf(wg[k], __uint_as_float(ml[k][2]), L); }
        L = sw16_add(L, L); L = sw32_add(L, L);
        const float scl = gt * __builtin_amdgcn_rcpf(L);
        *(volatile LAS u32x4*)(lds + wla) = u32x4{cvtpk(wg[0] * scl, wg[1] * scl), cvtpk(wg[2] * scl, wg[3] * scl), cvtpk(wg[4] * scl, wg[5] * scl), cvtpk(wg[6] * scl, wg[7] * scl)};
        flag[9] = 1;
        ST_OUT();
    }
    asm volatile("" : "+s"(wv));
    if (wv == 1) {
        u32 oa = ((lane >> 4) * 2 + ((lane >> 3) & 1)) * 8192 + (lane & 7) * 16;
        u32 pa = L_P + ((lane >> 2) & 3) * 320 + (lane >> 4) * 16;
        u32 l16 = lane * 16;
        PIN(oa); PIN(pa); PIN(l16);
        const rsrc_t rv = mk(a.v + kvoff, 32768);
        u32 ct = 0;
        if (PRET) { ct = __builtin_amdgcn_raw_buffer_load_b32(rv, 0, 0, 2); SB(); }
        if (PREW & 2) { bst(0.f, 0.f, 0, ws, oa, WS_O + g * 65536 + s * 256); bst(0.f, 0.f, 0, ws, oa + 128, WS_O + g * 65536 + s * 256); }
        u32x4 v[32];
#define VLD(c) do { prog_wait<(thr(56 + (c)) <= 56 ? thr(56 + (c)) : 0)>(); if (thr(56 + (c)) > 56) asm volatile("s_waitcnt vmcnt(%0)" :: "n"((c) - (thr(56 + (c)) - 56)) : "memory"); SB(); \
        _Pragma("unroll") for (int i = (c); i < (c) + 4; ++i) v[i] = bnt(rv, l16 + (i >> 3) * 1024, (i & 7) * 4096); SB(); } while (0)
        VLD(0); ST(1); VLD(4); VLD(8); VLD(12); VLD(16); VLD(20); VLD(24); VLD(28);
        flag[4] = 1;
        u32 tag = flag[10]; PIN(tag); PIN(ct);
        ST(2);
        u32x4 t0[8], t1[8], t2[8], t3[8], pf[4];
        prog_post<28, 60>(); prog_post<24, 64>();
        transpose8(v, t0);
#pragma unroll
        for (int i = 0; i < 8; ++i) PIN(t0[i]);
        prog_post<20, 68>(); prog_post<16, 72>();
        transpose8(v + 8, t1);
#pragma unroll
        for (int i = 0; i < 8; ++i) PIN(t1[i]);
        prog_post<12, 76>(); prog_post<8, 88>();
        transpose8(v + 16, t2);
#pragma unroll
        for (int i = 0; i < 8; ++i) PIN(t2[i]);
        SB();
#pragma nounroll
        for (u32 n = 0; n < SPIN_CAP * 16; ++n) {
            const u32 f = flag[5];
#pragma unroll
            for (int i = 0; i < 4; ++i) pf[i] = *(volatile LAS u32x4*)(lds + pa + i * 64);
            if (__builtin_amdgcn_readfirstlane(f)) break;
        }
        ST(3);
        f32x4 o[8];
#pragma unroll
        for (int e = 0; e < 8; ++e) { o[e] = mfma(pf[0], t0[e], f32x4{0.f, 0.f, 0.f, 0.f}); }
#pragma unroll
        for (int e = 0; e < 8; ++e) o[e] = mfma(pf[1], t1[e], o[e]);
#pragma unroll
        for (int e = 0; e < 8; ++e) o[e] = mfma(pf[2], t2[e], o[e]);
        transpose8(v + 24, t3);
        ST(4);
#pragma unroll
        for (int e = 0; e < 8; ++e) o[e] = mfma(pf[3], t3[e], o[e]);
#pragma unroll
        for (int k = 0; k < 2; ++k) bstu(cvtpk(o[4 * k][0], o[4 * k + 1][0]), cvtpk(o[4 * k + 2][0], o[4 * k + 3][0]), tag, ws, oa + k * 128, WS_O + g * 65536 + s * 256);
        SB();
        ST(10);
        __builtin_amdgcn_s_sleep(DELAY_O);
        flag[6] = 1;
        ST_OUT();
    }
    asm volatile("" : "+s"(wv));
    if (wv == 2) {
        const int kgrp = s >> 2, r8 = lane >> 3;
        const int c0 = 224 * g - 32 * (g & 1) + 64 * (s & 3);
        const int j = g * 32 + s, col = 128 * (j >> 6) + 8 * (j & 15) + 2 * ((j >> 4) & 3);
        const u32x4 xa = *(const GAS u32x4*)(a.x + lane * 16), gw = *(const GAS u32x4*)(a.gw + lane * 16);
        const u32x4 xr = *(const GAS u32x4*)(a.x + (kgrp * 64 + r8 * 8) * 2);
        const int pr = (lane & 15) < 14 ? (lane & 15) : 13;
        const u32 b1w = *(const GAS u32*)(a.b1 + (rho * 14 + pr) * 4);
        const u32 b2w = __builtin_amdgcn_raw_buffer_load_b32(mk(a.b2, 1024), 0, col * 2, 0);
        SB();
        const rsrc_t r1 = mk(a.w1 + kgrp * (64 * 3584) + c0 * 2, 64 * 3584 - c0 * 2);
        u32 ct = 0, cu = 0;
        if (PRET) { ct = __builtin_amdgcn_raw_buffer_load_b32(r1, (lane & 1) * (63 * 3584) + 64, 0, 2); cu = __builtin_amdgcn_raw_buffer_load_b32(mk(a.out, 1024), 0, 0, 2); SB(); }
        const int cl = c0 + 8 * (lane & 7) - 224 * g;
        u32 wa = (cl < 0 || cl >= 224) ? OOB : r8 * (8 * 3584) + (lane & 7) * 16;
        u32 fsa = (lane & 8) ? OOB : ((s & 3) * 32 + 4 * (lane & 7) + 2 * (lane >> 5) + ((lane >> 4) & 1)) * 16;
        u32 fa = (lane >> 4) * 2048 + ((s >> 2) * 14 + 16 * (g & 1) + pr) * 16;
        u32 ha = lane < 14 ? L_A + lane * 4 : L_DUMMY;
        u32 ya = lane * 1024, outa = (lane & 31) ? OOB : (lane >> 4), epa = (b | lane) ? OOB : 0;
        PIN(wa); PIN(fsa); PIN(fa); PIN(ha); PIN(ya); PIN(outa); PIN(epa);
        u32x4 w[8];
        prog_wait<(52 - WW > 24 ? 52 - WW : 24)>();
#pragma unroll
        for (int i = 0; i < 4; ++i) w[i] = bnt(r1, wa, i * 3584);
        SB();
        prog_wait<56 - WW>();
#pragma unroll
        for (int i = 4; i < 8; ++i) w[i] = bnt(r1, wa, i * 3584);
        SB();
        ST(2);
        const float gt = gate_of(xa, gw);
        flag[12] = __float_as_uint(gt);
        flag[13] = 1;
        float g1 = 1.f - gt, bb0 = bf_lo(b1w), bb1 = bf_hi(b1w);
        float bias = g1 * (lane < 32 ? bf_lo(b2w) : bf_hi(b2w));
        u32 tag = flag[10];
        PIN(ct); PIN(cu); PIN(g1); PIN(bb0); PIN(bb1); PIN(bias); PIN(tag);
        SB();
        float acc[8] = {0.f, 0.f, 0.f, 0.f, 0.f, 0.f, 0.f, 0.f};
#pragma unroll
        for (int i = 0; i < 8; ++i) {
            if (i == 3) prog_post<4, 52>();
            if (i == 7) prog_post<0, 56>();
            row_fma(acc, w[i], xr[i >> 1], i & 1);
            SB();
        }
        float u0, u1;
        reduce8(acc, u0, u1);
        bst(u0, u1, tag, ws, fsa, WS_F + g * 16384 + kgrp * 2048);
        SB();
        ST(3);
        lds_wait<F_TRIG * 4>();
        __builtin_amdgcn_s_sleep(DELAY_F);
        u32x4 f0 = bld(ws, fa, WS_F + g * 16384), f1 = bld(ws, fa, WS_F + g * 16384 + 8192);
        SB();
        ST(10);
        if (__builtin_expect(__builtin_amdgcn_ballot_w64(chk(chk(0, f0, tag), f1, tag) != 0) != 0, 0)) {
#pragma nounroll
            for (u32 n = 1; n < SPIN_CAP; ++n) {
                MEMBAR();
                f0 = bld(ws, fa, WS_F + g * 16384); f1 = bld(ws, fa, WS_F + g * 16384 + 8192);
                CNT(8, n);
                if (__builtin_amdgcn_ballot_w64(chk(chk(0, f0, tag), f1, tag) != 0) == 0) break;
            }
        }
        ST(4);
        float h0 = __uint_as_float(f0[0]) + __uint_as_float(f1[0]), h1 = __uint_as_float(f0[2]) + __uint_as_float(f1[2]);
        h0 = sw16_add(h0, h0); h1 = sw16_add(h1, h1);
        h0 = sw32_add(h0, h0) + bb0; h1 = sw32_add(h1, h1) + bb1;
        constexpr float GC = -2.f * 0.7978845608028654f * 1.4426950408889634f;
        h0 = h0 * __builtin_amdgcn_rcpf(1.f + __builtin_amdgcn_exp2f(GC * fmaf(0.044715f * h0 * h0, h0, h0)));
        h1 = h1 * __builtin_amdgcn_rcpf(1.f + __builtin_amdgcn_exp2f(GC * fmaf(0.044715f * h1 * h1, h1, h1)));
        *(volatile LAS u32*)(lds + ha) = cvtpk(h0 * g1, h1 * g1);
        flag[7] = 1;
        SB();
        ST(5);
        u32x4 y;
        if (PRIME & 2) { y = bld(ws, ya, WS_Y + (j >> 6) * 65536 + (j & 63) * 16); PIN(y); }
        lds_wait<32>();
#pragma nounroll
        for (u32 n = 0; n < SPIN_CAP; ++n) {
            MEMBAR();
            const u32x4 yn = bld(ws, ya, WS_Y + (j >> 6) * 65536 + (j & 63) * 16);
            const bool ok = ya != OOB && chk(0, yn, tag) == 0;
            if (ok) { y = yn; ya = OOB; }
            const u32 bad = ya != OOB;
            CNT(9, n); STF(7, n);
            if (__builtin_amdgcn_ballot_w64(bad != 0) == 0) break;
        }
        ST(6);
        float r = sw32_add(__uint_as_float(y[0]), __uint_as_float(y[2]));
        r = sw16_add(r, r); r += dpp<ROR8>(r); r += dpp<ROR4>(r); r += dpp<XOR2>(r); r += dpp<XOR1>(r);
        r += bias;
        __builtin_amdgcn_raw_buffer_store_b16((unsigned short)cvtpk(r, r), mk(a.out + col * 2, 4), outa, 0, 0);
        __builtin_amdgcn_raw_buffer_store_b32(tag, ws, epa, WS_EPOCH, 16);
        ST_OUT();
    }
    asm volatile("" : "+s"(wv));
    if (wv == 3) {
        const int kg = lane >> 4;
        const rsrc_t r2 = mk(a.w2 + rho * (28 * 1024) + cq * 256, 28 * 1024 - cq * 256);
        const rsrc_t ro = mk(a.wo + rho * (64 * 1024) + cq * 256, 64 * 1024 - cq * 256);
        u32 ct = 0, cu = 0;
        if (PRET) { ct = __builtin_amdgcn_raw_buffer_load_b32(r2, 0, 0, 2); cu = __builtin_amdgcn_raw_buffer_load_b32(ro, 0, 0, 2); SB(); }
        u32 w2a = kg * 8192 + (lane & 15) * 16, w2b = kg == 3 ? w2a - 4096 : w2a;
        u32 woa = (32 * (kg & 1) + 4 * (kg >> 1)) * 1024 + (lane & 15) * 16;
        u32 oga = kg * 2048 + (lane & 15) * 16, l16 = lane * 16, aa = L_A + kg * 16;
        PIN(w2a); PIN(w2b); PIN(woa); PIN(oga); PIN(l16); PIN(aa);
        if (PREW & 8) bst(0.f, 0.f, 0, ws, l16, WS_Y + cq * 65536 + rho * 1024);
        u32x4 r[8], u[16], t2[8], ta[8], tb[8];
        lds_wait<16>();
#pragma unroll
        for (int c = 0; c < 8; c += 4) {
            if (c == 0) prog_wait<thr(88)>(); else prog_wait<thr(92)>();
#pragma unroll
            for (int i = c; i < c + 4; ++i) r[i] = bnt(r2, (i < 4 ? w2a : w2b) + (i & 3) * 1024, (i >> 2) * 4096);
            SB();
        }
        ST(1);
#define ULD(c) do { prog_wait<(thr(96 + (c)) < 80 ? thr(96 + (c)) : 88)>(); _Pragma("unroll") for (int i = (c); i < (c) + 4; ++i) u[i] = bnt(ro, woa + (2 * (i >> 3) + ((i >> 2) & 1)) * 1024, (i & 3) * 8192); SB(); } while (0)
        ULD(0); ULD(4); ULD(8); ULD(12);
        ST(2);
        u32 tag = flag[10];
        PIN(tag); PIN(cw); PIN(cp); PIN(ct); PIN(cu);
        transpose8(r, t2);
#pragma unroll
        for (int i = 0; i < 8; ++i) PIN(t2[i]);
        transpose8(u, ta);
        transpose8(u + 8, tb);
#pragma unroll
        for (int i = 0; i < 8; ++i) { PIN(ta[i]); PIN(tb[i]); }
        if (PREY) bst(0.f, 0.f, tag - 1, ws, l16, WS_Y + cq * 65536 + rho * 1024);
        ST(5);
        lds_wait<24>();
        if (GATE_W) lds_wait<36>();
        ST(4);
        u32x4 og[8];
#pragma unroll
        for (int k = 0; k < 8; ++k) og[k] = bld(ws, oga + k * 256, WS_O + rho * 8192);
        SB();
        lds_wait<28>();
        const u32x4 af = *(volatile LAS u32x4*)(lds + aa);
        f32x4 d[8];
#pragma unroll
        for (int e = 0; e < 8; ++e) { d[e] = mfma(af, t2[e], f32x4{0.f, 0.f, 0.f, 0.f}); PIN(d[e]); }
        if (!GATE_W) lds_wait<36>();
        u32x4 wf = *(volatile LAS u32x4*)(lds + aa + (L_W - L_A));
        PIN(wf);
        SB();
        ST(3);
        {
            u32 bad = 0;
#pragma unroll
            for (int k = 0; k < 8; ++k) bad = chk(bad, og[k], tag);
            ST(10);
            if (__builtin_expect(__builtin_amdgcn_ballot_w64(bad != 0) != 0, 0)) {
#pragma nounroll
                for (u32 n = 1; n < SPIN_CAP; ++n) {
                    MEMBAR();
#pragma unroll
                    for (int k = 0; k < 8; ++k) og[k] = bld(ws, oga + k * 256, WS_O + rho * 8192);
                    bad = 0;
#pragma unroll
                    for (int k = 0; k < 8; ++k) bad = chk(bad, og[k], tag);
                    CNT(9, n);
                    if (__builtin_amdgcn_ballot_w64(bad != 0) == 0) break;
                }
            }
        }
        ST(7);
        f32x4 c[4];
#pragma unroll
        for (int e = 0; e < 4; ++e) {
            u32x4 ot;
#pragma unroll
            for (int p = 0; p < 4; ++p) ot[p] = __builtin_amdgcn_perm(og[2 * p + 1][e & 2], og[2 * p][e & 2], (e & 1) ? 0x07060302u : 0x05040100u);
            c[e] = mfma(ot, wf, f32x4{0.f, 0.f, 0.f, 0.f});
        }
        const u32x4 a0 = {cvtpk(c[0][0], c[0][1]), cvtpk(c[0][2], c[0][3]), cvtpk(c[1][0], c[1][1]), cvtpk(c[1][2], c[1][3])};
        const u32x4 a1 = {cvtpk(c[2][0], c[2][1]), cvtpk(c[2][2], c[2][3]), cvtpk(c[3][0], c[3][1]), cvtpk(c[3][2], c[3][3])};
#pragma unroll
        for (int e = 0; e < 8; ++e) d[e] = mfma(a0, ta[e], d[e]);
#pragma unroll
        for (int e = 0; e < 8; ++e) d[e] = mfma(a1, tb[e], d[e]);
        const bool k0 = kg & 1, k1 = kg & 2;
        const float ya0 = k0 ? d[2][0] : d[0][0], yb0 = k0 ? d[6][0] : d[4][0], ya1 = k0 ? d[3][0] : d[1][0], yb1 = k0 ? d[7][0] : d[5][0];
        const float y0 = k1 ? yb0 : ya0, y1 = k1 ? yb1 : ya1;
        bst(y0, y1, tag, ws, l16, WS_Y + cq * 65536 + rho * 1024);
        SB();
        ST(6);
        __builtin_amdgcn_s_sleep(DELAY_Y);
        flag[8] = 1;
        ST_OUT();
    }
}

extern "C" void kernel_launch(void* const* d_in, const int* in_sizes, int n_in, void* d_out, int out_size, void* d_ws, size_t ws_size, hipStream_t stream) {
    Args a;
    a.x = (const char*)d_in[0]; a.k = (const char*)d_in[1]; a.v = (const char*)d_in[2]; a.wq = (const char*)d_in[3]; a.wo = (const char*)d_in[4];
    a.w1 = (const char*)d_in[6]; a.b1 = (const char*)d_in[7]; a.w2 = (const char*)d_in[8]; a.b2 = (const char*)d_in[9]; a.gw = (const char*)d_in[10];
    a.out = (char*)d_out; a.ws = (char*)d_ws + (WSOFF ? ws_size - (8u << 20) : 0);
    gated_mix_kernel<<<256, 256, 0, stream>>>(a.wq, a.x, a.k, a.ws, a.v, a.w1, a.gw, a.wo, a.b1, a.w2, a.b2, a.out);
}
```

```cpp
#include <hip/hip_runtime.h>
#include <stdint.h>

typedef unsigned u32;
typedef unsigned long long u64;
typedef u32 u32x4 __attribute__((ext_vector_type(4)));
typedef float f32x4 __attribute__((ext_vector_type(4)));
typedef __bf16 bf16x8 __attribute__((ext_vector_type(8)));
typedef __bf16 bf16x2 __attribute__((ext_vector_type(2)));
#define GAS __attribute__((address_space(1)))
#define LAS __attribute__((address_space(3)))
#define SB() __builtin_amdgcn_sched_barrier(0)
#define MEMBAR() asm volatile("" ::: "memory")

#define GATHER_AT 1
#define VHOLD 16
#define DELAY_ML 20
#define DELAY_O 16
#define DELAY_Y 16
#define WO_FIRST 0
#define DELAY_F 8
#define F_TRIG 5
#define K0 32
#define REL_F 29
#define REL_T 9
#define VWIN 32
#define PRIME 0
#define REL_V 13
#define PREY 0
#define W2_LATE 1
#define GATE_W 1
#define WW 28
#define GPOS 28
#define NTP 0
#define WW2 WW
#define PSW 72
#define PREW 0
#define WSOFF 0

constexpr int SK = 65536, WIN = 4096;
constexpr u32 SPIN_CAP = 1u << 16;
constexpr u32 WS_EPOCH = 0;
constexpr u32 WS_Q = 4096;
constexpr u32 WS_ML = WS_Q + 8 * 16384;
constexpr u32 WS_F = WS_ML + 32768;
constexpr u32 WS_O = WS_F + 64 * 2048;
constexpr u32 WS_Y = WS_O + (1u << 19);
constexpr u32 WS_END = WS_Y + (1u << 18);
constexpr u32 WS_STAMP = 4u << 20;
constexpr int L_FLAG = 0;
constexpr int L_DUMMY = 60;
constexpr int L_A = 64;
constexpr int L_W = 384;
constexpr int L_Z = 128;
constexpr int L_Q = 512;
constexpr int L_P = L_Q + 1280;
constexpr int L_END = L_P + 1280;

struct Args {
    const char* x; const char* k; const char* v; const char* wq; const char* wo;
    const char* w1; const char* b1; const char* w2; const char* b2; const char* gw;
    char* out; char* ws;
};

#define ST(i)
#define CNT(i, n)
#define STF(i, n)
#define ST_DECL
#define ST_OUT()

__device__ __forceinline__ u32x4 ldnt(const char* p) { return __builtin_nontemporal_load((const GAS u32x4*)p); }
typedef __amdgpu_buffer_rsrc_t rsrc_t;
__device__ __forceinline__ rsrc_t mk(const char* p, u32 n) { return __builtin_amdgcn_make_buffer_rsrc((void*)p, 0, n, 0x00020000); }
__device__ __forceinline__ u32x4 bnt(rsrc_t r, u32 v, u32 s) { return __builtin_amdgcn_raw_buffer_load_b128(r, v, s, 2); }
__device__ __forceinline__ u32x4 bld(rsrc_t r, u32 v, u32 s) { return __builtin_amdgcn_raw_buffer_load_b128(r, v, s, 16); }
__device__ __forceinline__ void bstu(u32 a, u32 b, u32 tag, rsrc_t r, u32 v, u32 s) { __builtin_amdgcn_raw_buffer_store_b128(u32x4{a, tag, b, tag}, r, v, s, 16); }
__device__ __forceinline__ u32 chk(u32 bad, u32x4 x, u32 tag) { bad |= x[1] ^ tag; asm volatile("" : "+v"(bad)); bad |= x[3] ^ tag; asm volatile("" : "+v"(bad)); return bad; }
__device__ __forceinline__ void bst(float a, float b, u32 tag, rsrc_t r, u32 v, u32 s) { __builtin_amdgcn_raw_buffer_store_b128(u32x4{__float_as_uint(a), tag, __float_as_uint(b), tag}, r, v, s, 16); }
#define PIN(x) asm volatile("" : "+v"(x))
constexpr u32 OOB = 0x80000000u;
constexpr u32 CODE_BYTES = 12800;
__device__ __forceinline__ float dot2(u32 a, u32 b, float c) { return __builtin_amdgcn_fdot2_f32_bf16(__builtin_bit_cast(bf16x2, a), __builtin_bit_cast(bf16x2, b), c, false); }
__device__ __forceinline__ u32 cvtpk(float lo, float hi) { bf16x2 r = {(__bf16)lo, (__bf16)hi}; return __builtin_bit_cast(u32, r); }
template <int CTRL> __device__ __forceinline__ float dpp(float x) { return __builtin_bit_cast(float, __builtin_amdgcn_mov_dpp(__builtin_bit_cast(int, x), CTRL, 0xf, 0xf, true)); }
constexpr int XOR1 = 0xB1, XOR2 = 0x4E, ROR4 = 0x124, ROR8 = 0x128;
__device__ __forceinline__ float sw16_add(float a, float b) { auto s = __builtin_amdgcn_permlane16_swap(__float_as_uint(a), __float_as_uint(b), false, false); return __uint_as_float(s[0]) + __uint_as_float(s[1]); }
__device__ __forceinline__ float sw32_add(float a, float b) { auto s = __builtin_amdgcn_permlane32_swap(__float_as_uint(a), __float_as_uint(b), false, false); return __uint_as_float(s[0]) + __uint_as_float(s[1]); }
__device__ __forceinline__ float fmx(float a, float b) { return __builtin_elementwise_maximum(a, b); }
__device__ __forceinline__ float sw16_max(float a) { auto s = __builtin_amdgcn_permlane16_swap(__float_as_uint(a), __float_as_uint(a), false, false); return fmx(__uint_as_float(s[0]), __uint_as_float(s[1])); }
__device__ __forceinline__ float sw32_max(float a) { auto s = __builtin_amdgcn_permlane32_swap(__float_as_uint(a), __float_as_uint(a), false, false); return fmx(__uint_as_float(s[0]), __uint_as_float(s[1])); }
__device__ __forceinline__ f32x4 mfma(u32x4 a, u32x4 b, f32x4 c) { return __builtin_amdgcn_mfma_f32_16x16x32_bf16(__builtin_bit_cast(bf16x8, a), __builtin_bit_cast(bf16x8, b), c, 0, 0, 0); }
__device__ __forceinline__ float bf_lo(u32 w) { return __uint_as_float(w << 16); }
__device__ __forceinline__ float bf_hi(u32 w) { return __uint_as_float(w & 0xffff0000u); }

__device__ __forceinline__ void transpose8(const u32x4* r, u32x4* t) {
#pragma unroll
    for (int p = 0; p < 4; ++p)
#pragma unroll
        for (int d = 0; d < 4; ++d) {
            t[2 * d][p] = __builtin_amdgcn_perm(r[2 * p + 1][d], r[2 * p][d], 0x05040100u);
            t[2 * d + 1][p] = __builtin_amdgcn_perm(r[2 * p + 1][d], r[2 * p][d], 0x07060302u);
        }
}
__device__ __forceinline__ void row_fma(float* acc, u32x4 w, u32 xpair, int odd) {
    const u32 xl = odd ? xpair >> 16 : xpair & 0xffffu, xh = odd ? xpair & 0xffff0000u : xpair << 16;
#pragma unroll
    for (int d = 0; d < 4; ++d) { acc[2 * d] = dot2(w[d], xl, acc[2 * d]); acc[2 * d + 1] = dot2(w[d], xh, acc[2 * d + 1]); }
}
__device__ __forceinline__ void reduce8(const float* acc, float& u0, float& u1) {
    const float s0 = sw32_add(acc[0], acc[4]), s1 = sw32_add(acc[1], acc[5]), s2 = sw32_add(acc[2], acc[6]), s3 = sw32_add(acc[3], acc[7]);
    u0 = sw16_add(s0, s2); u1 = sw16_add(s1, s3);
    u0 += dpp<ROR8>(u0); u1 += dpp<ROR8>(u1);
}
__device__ __forceinline__ float gate_of(u32x4 x, u32x4 gw) {
    float s = 0.f;
#pragma unroll
    for (int d = 0; d < 4; ++d) s = dot2(x[d], gw[d], s);
    s += dpp<XOR1>(s); s += dpp<XOR2>(s); s += dpp<ROR4>(s); s += dpp<ROR8>(s);
    s = sw16_add(s, s); s = sw32_add(s, s);
    return __builtin_amdgcn_rcpf(1.f + __builtin_amdgcn_exp2f(-1.4426950408889634f * s));
}
template <int OFF> __device__ __forceinline__ void lds_wait() {
    u32 t, n, c;
    asm volatile("s_mov_b32 %1, 0x100000\n\tv_mov_b32 %0, 0\n"
                 "1:\n\tds_read_b32 %0, %0 offset:%3\n\ts_waitcnt lgkmcnt(0)\n\tv_readfirstlane_b32 %2, %0\n\ts_cmp_lg_u32 %2, 0\n\ts_cbranch_scc1 2f\n\t"
                 "s_sleep 1\n\ts_sub_u32 %1, %1, 1\n\ts_cbranch_scc0 1b\n"
                 "2:" : "=&v"(t), "=&s"(n), "=&s"(c) : "n"(OFF) : "memory", "scc");
}
template <int THR> __device__ __forceinline__ void prog_wait() {
    if (THR <= 0) return;
    u32 t, z, n, c;
    asm volatile("s_mov_b32 %1, 0x200000\n\tv_mov_b32 %3, 0\n"
                 "1:\n\tds_read_b32 %0, %3 offset:56\n\ts_waitcnt lgkmcnt(0)\n\tv_readfirstlane_b32 %2, %0\n\ts_cmp_ge_u32 %2, %4\n\ts_cbranch_scc1 2f\n\t"
                 "s_sub_u32 %1, %1, 1\n\ts_cbranch_scc0 1b\n"
                 "2:" : "=&v"(t), "=&s"(n), "=&s"(c), "=&v"(z) : "n"(THR) : "memory", "scc");
}
template <int N, int V> __device__ __forceinline__ void prog_post() {
    u32 z, v;
    asm volatile("v_mov_b32 %0, 0\n\tv_mov_b32 %1, %3\n\ts_waitcnt vmcnt(%2)\n\tds_max_u32 %0, %1 offset:56" : "=&v"(z), "=&v"(v) : "n"(N), "n"(V) : "memory");
}
constexpr int thr(int p) { return p < PSW ? p + 4 - WW : (p + 4 - WW2 > 20 + GPOS ? p + 4 - WW2 : (p + 4 - WW < 20 + GPOS ? p + 4 - WW : 20 + GPOS)); }
#define BARRIER() do { asm volatile("s_waitcnt lgkmcnt(0)" ::: "memory"); __builtin_amdgcn_s_barrier(); MEMBAR(); } while (0)

__global__ void __launch_bounds__(256, 2) gated_mix_kernel(Args a) {
    __shared__ __attribute__((aligned(16))) char lds_[L_END];
    LAS char* lds = (LAS char*)lds_;
    volatile LAS u32* flag = (volatile LAS u32*)(lds + L_FLAG);
    const int b = blockIdx.x, g = b & 7, s = b >> 3, lane = threadIdx.x & 63;
    int wv = __builtin_amdgcn_readfirstlane(threadIdx.x >> 6);
    ST_DECL; ST(0);
    u64 pc; asm volatile("s_getpc_b64 %0" : "=s"(pc));
    if (threadIdx.x < L_Q / 4) *(volatile LAS u32*)(lds + threadIdx.x * 4) = 0;
    asm volatile("" :: "s"(a.x), "s"(a.k), "s"(a.v), "s"(a.wq), "s"(a.wo), "s"(a.w1), "s"(a.b1), "s"(a.w2), "s"(a.b2), "s"(a.gw), "s"(a.out), "s"(a.ws));
    BARRIER();
    const rsrc_t ws = mk(a.ws, WS_END);
    const u32 kvoff = (u32)(g * SK + (SK - WIN) + s * 128) * 256u;
    u32 cw = __builtin_amdgcn_raw_buffer_load_b32(mk((const char*)(pc & ~255ull), wv == 3 ? CODE_BYTES : 0), (lane & 15) * 128 + (lane >> 4) * 8192, (s & 3) * 2048, 0);
    SB();
    u32 cp = 0;
    if (NTP) {
        const int jj = g * 32 + s;
        u32 pa = OOB;
        if ((NTP & 1) && lane < 16) pa = WS_O + (8 * g + (s >> 2)) * 8192 + ((s & 3) * 16 + lane) * 128;
        if ((NTP & 2) && lane >= 16 && lane < 24) pa = WS_Y + (jj >> 6) * 65536 + ((jj & 7) * 8 + lane - 16) * 1024 + ((jj & 63) >> 3) * 128;
        if ((NTP & 4) && lane == 24) pa = WS_ML + g * 4096 + s * 128;
        if ((NTP & 8) && lane >= 28 && lane < 32) pa = WS_F + g * 16384 + (s * 4 + lane - 28) * 128;
        if ((NTP & 16) && lane >= 32 && lane < 36) pa = WS_Q + g * 16384 + (s * 4 + lane - 32) * 128;
        cp = __builtin_amdgcn_raw_buffer_load_b32(ws, wv == 3 ? pa : OOB, 0, NTP & 32 ? 0 : 2);
        SB();
    }
    const int rho = 8 * g + (s >> 2), cq = s & 3;

    if (wv == 0) {
        const int kq = s & 3, r8 = lane >> 3;
        u32 tag = __builtin_amdgcn_raw_buffer_load_b32(ws, 0, WS_EPOCH, 16);
        u32x4 xr[2], w[16], kf[32];
        const char* xp = a.x + (kq * 128 + r8 * 16) * 2;
        xr[0] = *(const GAS u32x4*)xp; xr[1] = *(const GAS u32x4*)(xp + 16);
        const rsrc_t rq = mk(a.wq + kq * (128 * 8192) + g * 1024 + (s >> 2) * 128, 128 * 8192);
        const u32 wo_ = r8 * (16 * 8192) + (lane & 7) * 16;
#pragma unroll
        for (int i = 0; i < 16; ++i) { w[i] = bnt(rq, wo_, i * 8192); SB(); }
        const int key3 = (lane >> 1) & 7;
        const u32 ko = key3 * 256 + (2 * (lane >> 4) + (lane & 1)) * 16;
        const rsrc_t rk = mk(a.k + kvoff, 32768);
#define KLD(i) bnt(rk, ko + (((i) >> 1) & 1) * 2048 + ((i) & 1) * 128, ((i) >> 2) * 4096)
#pragma unroll
        for (int i = 0; i < WW - 16; ++i) kf[i] = KLD(i);
        SB();
        ST(1);
        const int hd = (lane >> 2) & 3, xb = (lane >> 1) & 1, yb = lane & 1;
        u32 qsa = (lane & 8) ? OOB : ((s >> 2) * 32 + 4 * (lane & 7) + 2 * (lane >> 5) + ((lane >> 4) & 1)) * 16;
        u32 qa0 = L_Q + hd * 320 + (2 * (lane >> 4) + yb) * 16, qa1 = xb ? qa0 : L_Z;
        qa0 = xb ? L_Z : qa0;
        u32 pwa = L_P + hd * 320 + (2 * xb + (lane >> 5)) * 64 + (2 * ((lane >> 4) & 1) + yb) * 16;
        u32 mla = (lane & 0x23) ? OOB : hd * 16 + (lane & 16) * 4;
        u32 l16 = lane * 16;
        PIN(qsa); PIN(qa0); PIN(qa1); PIN(pwa); PIN(mla); PIN(l16);
        SB();
        float acc[8] = {0.f, 0.f, 0.f, 0.f, 0.f, 0.f, 0.f, 0.f};
#pragma unroll
        for (int i = 0; i < 16; ++i) {
            if (WW - 16 + i < 32 && i < 15) { PIN(w[i]); kf[WW - 16 + i] = KLD(WW - 16 + i); SB(); }
            row_fma(acc, w[i], xr[i >> 3][(i >> 1) & 3], i & 1);
            SB();
        }
        float u0, u1;
        reduce8(acc, u0, u1);
        tag += 1;
        ST(2);
        bst(u0, u1, tag, ws, qsa, WS_Q + g * 16384 + kq * 4096);
        flag[10] = tag;
        SB();
        constexpr int NI = WW - 1 < 32 ? WW - 1 : 32;
        if (NI < 32) {
            asm volatile("s_waitcnt vmcnt(%0)" :: "n"(NI + 1 - (32 - NI)) : "memory");
#pragma unroll
            for (int i = NI; i < 32; ++i) kf[i] = KLD(i);
            SB();
        }
#define KP(c) if ((c) <= GPOS) prog_post<32 - (c) + ((c) <= NI ? 1 : 0), 16 + (c)>()
        KP(8); KP(12); KP(16); KP(20); KP(24); KP(28); KP(32);
        ST(3);
        u32x4 qg[16];
#pragma nounroll
        for (u32 n = 0; n < SPIN_CAP; ++n) {
            MEMBAR();
#pragma unroll
            for (int i = 0; i < 16; ++i) qg[i] = bld(ws, l16 + (i >> 2) * 1024, WS_Q + g * 16384 + (i & 3) * 4096);
            SB();
#define KQ(c) if ((c) > GPOS) prog_post<48 - (c) + ((c) <= NI ? 1 : 0), 16 + (c)>()
            KQ(12); KQ(16); KQ(20); KQ(24); KQ(28); KQ(32);
            SB();
            u32 bad = 0;
#pragma unroll
            for (int i = 0; i < 16; ++i) bad = chk(bad, qg[i], tag);
            CNT(8, n); STF(7, n);
            if (__builtin_amdgcn_ballot_w64(bad != 0) == 0) break;
        }
        ST(4);
#pragma unroll
        for (int t = 0; t < 4; ++t) {
            const float q0 = (__uint_as_float(qg[4 * t][0]) + __uint_as_float(qg[4 * t + 1][0])) + (__uint_as_float(qg[4 * t + 2][0]) + __uint_as_float(qg[4 * t + 3][0]));
            const float q1 = (__uint_as_float(qg[4 * t][2]) + __uint_as_float(qg[4 * t + 1][2])) + (__uint_as_float(qg[4 * t + 2][2]) + __uint_as_float(qg[4 * t + 3][2]));
            *(volatile LAS u32*)(lds + L_Q + t * 320 + lane * 4) = cvtpk(q0, q1);
        }
        u32x4 qf[2][2];
        qf[0][0] = *(volatile LAS u32x4*)(lds + qa0); qf[0][1] = *(volatile LAS u32x4*)(lds + qa0 + 128);
        qf[1][0] = *(volatile LAS u32x4*)(lds + qa1); qf[1][1] = *(volatile LAS u32x4*)(lds + qa1 + 128);
        SB();
        float sc[8];
#pragma unroll
        for (int n = 0; n < 8; ++n) {
            f32x4 d = {0.f, 0.f, 0.f, 0.f};
            d = mfma(kf[4 * n], qf[0][0], d); d = mfma(kf[4 * n + 1], qf[0][1], d);
            d = mfma(kf[4 * n + 2], qf[1][0], d); d = mfma(kf[4 * n + 3], qf[1][1], d);
            const float p = yb ? d[3] : d[0], q = yb ? d[1] : d[2];
            sc[n] = p + dpp<XOR1>(q);
        }
        ST(5);
        constexpr float C2 = 0.08838834764831845f * 1.4426950408889634f;
        float m = fmx(fmx(fmx(sc[0], sc[1]), fmx(sc[2], sc[3])), fmx(fmx(sc[4], sc[5]), fmx(sc[6], sc[7])));
        m = fmx(m, dpp<XOR1>(m)); m = fmx(m, dpp<XOR2>(m)); m = sw16_max(m); m = sw32_max(m);
        m *= C2;
        float p[8];
#pragma unroll
        for (int n = 0; n < 8; ++n) p[n] = __builtin_amdgcn_exp2f(fmaf(sc[n], C2, -m));
        *(volatile LAS u32x4*)(lds + pwa) = u32x4{cvtpk(p[0], p[1]), cvtpk(p[2], p[3]), cvtpk(p[4], p[5]), cvtpk(p[6], p[7])};
        flag[5] = 1;
        SB();
        ST(6);
        float l = ((p[0] + p[1]) + (p[2] + p[3])) + ((p[4] + p[5]) + (p[6] + p[7]));
        l += dpp<XOR1>(l); l += dpp<XOR2>(l); l = sw16_add(l, l); l = sw32_add(l, l);
        bst(m, l, tag, ws, mla, WS_ML + g * 4096 + s * 128);
        SB();
        ST(9);
        u32 mga = (lane >> 4) * 1024, wla = L_W + (lane >> 4) * 16;
        lds_wait<52>();
        float gt = __uint_as_float(flag[12]);
        PIN(mga); PIN(wla); PIN(gt);
        __builtin_amdgcn_s_sleep(DELAY_ML);
        u32x4 ml[8];
#pragma nounroll
        for (u32 n = 0; n < SPIN_CAP; ++n) {
            MEMBAR();
#pragma unroll
            for (int k = 0; k < 8; ++k) ml[k] = bld(ws, mga + k * 128, WS_ML + g * 4096 + (s >> 3) * 16);
            u32 bad = 0;
#pragma unroll
            for (int k = 0; k < 8; ++k) bad = chk(bad, ml[k], tag);
            if (__builtin_amdgcn_ballot_w64(bad != 0) == 0) break;
        }
        ST(10);
        float M = fmx(fmx(fmx(__uint_as_float(ml[0][0]), __uint_as_float(ml[1][0])), fmx(__uint_as_float(ml[2][0]), __uint_as_float(ml[3][0]))),
                      fmx(fmx(__uint_as_float(ml[4][0]), __uint_as_float(ml[5][0])), fmx(__uint_as_float(ml[6][0]), __uint_as_float(ml[7][0]))));
        M = sw16_max(M); M = sw32_max(M);
        float wg[8], L = 0.f;
#pragma unroll
        for (int k = 0; k < 8; ++k) { wg[k] = __builtin_amdgcn_exp2f(__uint_as_float(ml[k][0]) - M); L = fmaf(wg[k], __uint_as_float(ml[k][2]), L); }
        L = sw16_add(L, L); L = sw32_add(L, L);
        const float scl = gt * __builtin_amdgcn_rcpf(L);
        *(volatile LAS u32x4*)(lds + wla) = u32x4{cvtpk(wg[0] * scl, wg[1] * scl), cvtpk(wg[2] * scl, wg[3] * scl), cvtpk(wg[4] * scl, wg[5] * scl), cvtpk(wg[6] * scl, wg[7] * scl)};
        flag[9] = 1;
        ST_OUT();
    }
    asm volatile("" : "+s"(wv));
    if (wv == 1) {
        u32 oa = ((lane >> 4) * 2 + ((lane >> 3) & 1)) * 8192 + (lane & 7) * 16;
        u32 pa = L_P + ((lane >> 2) & 3) * 320 + (lane >> 4) * 16;
        u32 l16 = lane * 16;
        PIN(oa); PIN(pa); PIN(l16);
        const rsrc_t rv = mk(a.v + kvoff, 32768);
        if (PREW & 2) { bst(0.f, 0.f, 0, ws, oa, WS_O + g * 65536 + s * 256); bst(0.f, 0.f, 0, ws, oa + 128, WS_O + g * 65536 + s * 256); }
        u32x4 v[32];
#define VLD(c) do { prog_wait<(thr(56 + (c)) <= 56 ? thr(56 + (c)) : 0)>(); if (thr(56 + (c)) > 56) asm volatile("s_waitcnt vmcnt(%0)" :: "n"((c) - (thr(56 + (c)) - 56)) : "memory"); SB(); \
        _Pragma("unroll") for (int i = (c); i < (c) + 4; ++i) v[i] = bnt(rv, l16 + (i >> 3) * 1024, (i & 7) * 4096); SB(); } while (0)
        VLD(0); ST(1); VLD(4); VLD(8); VLD(12); VLD(16); VLD(20); VLD(24); VLD(28);
        flag[4] = 1;
        u32 tag = flag[10]; PIN(tag);
        ST(2);
        u32x4 t0[8], t1[8], t2[8], t3[8], pf[4];
        prog_post<28, 60>(); prog_post<24, 64>();
        transpose8(v, t0);
#pragma unroll
        for (int i = 0; i < 8; ++i) PIN(t0[i]);
        prog_post<20, 68>(); prog_post<16, 72>();
        transpose8(v + 8, t1);
#pragma unroll
        for (int i = 0; i < 8; ++i) PIN(t1[i]);
        prog_post<12, 76>(); prog_post<8, 88>();
        transpose8(v + 16, t2);
#pragma unroll
        for (int i = 0; i < 8; ++i) PIN(t2[i]);
        SB();
#pragma nounroll
        for (u32 n = 0; n < SPIN_CAP * 16; ++n) {
            const u32 f = flag[5];
#pragma unroll
            for (int i = 0; i < 4; ++i) pf[i] = *(volatile LAS u32x4*)(lds + pa + i * 64);
            if (__builtin_amdgcn_readfirstlane(f)) break;
        }
        ST(3);
        f32x4 o[8];
#pragma unroll
        for (int e = 0; e < 8; ++e) { o[e] = mfma(pf[0], t0[e], f32x4{0.f, 0.f, 0.f, 0.f}); }
#pragma unroll
        for (int e = 0; e < 8; ++e) o[e] = mfma(pf[1], t1[e], o[e]);
#pragma unroll
        for (int e = 0; e < 8; ++e) o[e] = mfma(pf[2], t2[e], o[e]);
        transpose8(v + 24, t3);
        ST(4);
#pragma unroll
        for (int e = 0; e < 8; ++e) o[e] = mfma(pf[3], t3[e], o[e]);
#pragma unroll
        for (int k = 0; k < 2; ++k) bstu(cvtpk(o[4 * k][0], o[4 * k + 1][0]), cvtpk(o[4 * k + 2][0], o[4 * k + 3][0]), tag, ws, oa + k * 128, WS_O + g * 65536 + s * 256);
        SB();
        ST(10);
        __builtin_amdgcn_s_sleep(DELAY_O);
        flag[6] = 1;
        ST_OUT();
    }
    asm volatile("" : "+s"(wv));
    if (wv == 2) {
        const int kgrp = s >> 2, r8 = lane >> 3;
        const int c0 = 224 * g - 32 * (g & 1) + 64 * (s & 3);
        const int j = g * 32 + s, col = 128 * (j >> 6) + 8 * (j & 15) + 2 * ((j >> 4) & 3);
        const u32x4 xa = *(const GAS u32x4*)(a.x + lane * 16), gw = *(const GAS u32x4*)(a.gw + lane * 16);
        const u32x4 xr = *(const GAS u32x4*)(a.x + (kgrp * 64 + r8 * 8) * 2);
        const int pr = (lane & 15) < 14 ? (lane & 15) : 13;
        const u32 b1w = *(const GAS u32*)(a.b1 + (rho * 14 + pr) * 4);
        const u32 b2w = __builtin_amdgcn_raw_buffer_load_b32(mk(a.b2, 1024), 0, col * 2, 0);
        SB();
        const rsrc_t r1 = mk(a.w1 + kgrp * (64 * 3584) + c0 * 2, 64 * 3584 - c0 * 2);
        const int cl = c0 + 8 * (lane & 7) - 224 * g;
        u32 wa = (cl < 0 || cl >= 224) ? OOB : r8 * (8 * 3584) + (lane & 7) * 16;
        u32 fsa = (lane & 8) ? OOB : ((s & 3) * 32 + 4 * (lane & 7) + 2 * (lane >> 5) + ((lane >> 4) & 1)) * 16;
        u32 fa = (lane >> 4) * 2048 + ((s >> 2) * 14 + 16 * (g & 1) + pr) * 16;
        u32 ha = lane < 14 ? L_A + lane * 4 : L_DUMMY;
        u32 ya = lane * 1024, outa = (lane & 31) ? OOB : (lane >> 4), epa = (b | lane) ? OOB : 0;
        PIN(wa); PIN(fsa); PIN(fa); PIN(ha); PIN(ya); PIN(outa); PIN(epa);
        u32x4 w[8];
        prog_wait<(52 - WW > 24 ? 52 - WW : 24)>();
#pragma unroll
        for (int i = 0; i < 4; ++i) w[i] = bnt(r1, wa, i * 3584);
        SB();
        prog_wait<56 - WW>();
#pragma unroll
        for (int i = 4; i < 8; ++i) w[i] = bnt(r1, wa, i * 3584);
        SB();
        ST(2);
        const float gt = gate_of(xa, gw);
        flag[12] = __float_as_uint(gt);
        flag[13] = 1;
        float g1 = 1.f - gt, bb0 = bf_lo(b1w), bb1 = bf_hi(b1w);
        float bias = g1 * (lane < 32 ? bf_lo(b2w) : bf_hi(b2w));
        u32 tag = flag[10];
        PIN(g1); PIN(bb0); PIN(bb1); PIN(bias); PIN(tag);
        SB();
        float acc[8] = {0.f, 0.f, 0.f, 0.f, 0.f, 0.f, 0.f, 0.f};
#pragma unroll
        for (int i = 0; i < 8; ++i) {
            if (i == 3) prog_post<4, 52>();
            if (i == 7) prog_post<0, 56>();
            row_fma(acc, w[i], xr[i >> 1], i & 1);
            SB();
        }
        float u0, u1;
        reduce8(acc, u0, u1);
        bst(u0, u1, tag, ws, fsa, WS_F + g * 16384 + kgrp * 2048);
        SB();
        ST(3);
        lds_wait<F_TRIG * 4>();
        __builtin_amdgcn_s_sleep(DELAY_F);
        u32x4 f0 = bld(ws, fa, WS_F + g * 16384), f1 = bld(ws, fa, WS_F + g * 16384 + 8192);
        SB();
        ST(10);
        if (__builtin_expect(__builtin_amdgcn_ballot_w64(chk(chk(0, f0, tag), f1, tag) != 0) != 0, 0)) {
#pragma nounroll
            for (u32 n = 1; n < SPIN_CAP; ++n) {
                MEMBAR();
                f0 = bld(ws, fa, WS_F + g * 16384); f1 = bld(ws, fa, WS_F + g * 16384 + 8192);
                CNT(8, n);
                if (__builtin_amdgcn_ballot_w64(chk(chk(0, f0, tag), f1, tag) != 0) == 0) break;
            }
        }
        ST(4);
        float h0 = __uint_as_float(f0[0]) + __uint_as_float(f1[0]), h1 = __uint_as_float(f0[2]) + __uint_as_float(f1[2]);
        h0 = sw16_add(h0, h0); h1 = sw16_add(h1, h1);
        h0 = sw32_add(h0, h0) + bb0; h1 = sw32_add(h1, h1) + bb1;
        constexpr float GC = -2.f * 0.7978845608028654f * 1.4426950408889634f;
        h0 = h0 * __builtin_amdgcn_rcpf(1.f + __builtin_amdgcn_exp2f(GC * fmaf(0.044715f * h0 * h0, h0, h0)));
        h1 = h1 * __builtin_amdgcn_rcpf(1.f + __builtin_amdgcn_exp2f(GC * fmaf(0.044715f * h1 * h1, h1, h1)));
        *(volatile LAS u32*)(lds + ha) = cvtpk(h0 * g1, h1 * g1);
        flag[7] = 1;
        SB();
        ST(5);
        u32x4 y;
        if (PRIME & 2) { y = bld(ws, ya, WS_Y + (j >> 6) * 65536 + (j & 63) * 16); PIN(y); }
        lds_wait<32>();
#pragma nounroll
        for (u32 n = 0; n < SPIN_CAP; ++n) {
            MEMBAR();
            const u32x4 yn = bld(ws, ya, WS_Y + (j >> 6) * 65536 + (j & 63) * 16);
            const bool ok = ya != OOB && chk(0, yn, tag) == 0;
            if (ok) { y = yn; ya = OOB; }
            const u32 bad = ya != OOB;
            CNT(9, n); STF(7, n);
            if (__builtin_amdgcn_ballot_w64(bad != 0) == 0) break;
        }
        ST(6);
        float r = sw32_add(__uint_as_float(y[0]), __uint_as_float(y[2]));
        r = sw16_add(r, r); r += dpp<ROR8>(r); r += dpp<ROR4>(r); r += dpp<XOR2>(r); r += dpp<XOR1>(r);
        r += bias;
        __builtin_amdgcn_raw_buffer_store_b16((unsigned short)cvtpk(r, r), mk(a.out + col * 2, 4), outa, 0, 0);
        __builtin_amdgcn_raw_buffer_store_b32(tag, ws, epa, WS_EPOCH, 16);
        ST_OUT();
    }
    asm volatile("" : "+s"(wv));
    if (wv == 3) {
        const int kg = lane >> 4;
        const rsrc_t r2 = mk(a.w2 + rho * (28 * 1024) + cq * 256, 28 * 1024 - cq * 256);
        const rsrc_t ro = mk(a.wo + rho * (64 * 1024) + cq * 256, 64 * 1024 - cq * 256);
        u32 w2a = kg * 8192 + (lane & 15) * 16, w2b = kg == 3 ? w2a - 4096 : w2a;
        u32 woa = (32 * (kg & 1) + 4 * (kg >> 1)) * 1024 + (lane & 15) * 16;
        u32 oga = kg * 2048 + (lane & 15) * 16, l16 = lane * 16, aa = L_A + kg * 16;
        PIN(w2a); PIN(w2b); PIN(woa); PIN(oga); PIN(l16); PIN(aa);
        if (PREW & 8) bst(0.f, 0.f, 0, ws, l16, WS_Y + cq * 65536 + rho * 1024);
        u32x4 r[8], u[16], t2[8], ta[8], tb[8];
        lds_wait<16>();
#pragma unroll
        for (int c = 0; c < 8; c += 4) {
            if (c == 0) prog_wait<thr(88)>(); else prog_wait<thr(92)>();
#pragma unroll
            for (int i = c; i < c + 4; ++i) r[i] = bnt(r2, (i < 4 ? w2a : w2b) + (i & 3) * 1024, (i >> 2) * 4096);
            SB();
        }
        ST(1);
#define ULD(c) do { prog_wait<(thr(96 + (c)) < 80 ? thr(96 + (c)) : 88)>(); _Pragma("unroll") for (int i = (c); i < (c) + 4; ++i) u[i] = bnt(ro, woa + (2 * (i >> 3) + ((i >> 2) & 1)) * 1024, (i & 3) * 8192); SB(); } while (0)
        ULD(0); ULD(4); ULD(8); ULD(12);
        ST(2);
        u32 tag = flag[10];
        PIN(tag); PIN(cw); PIN(cp);
        transpose8(r, t2);
#pragma unroll
        for (int i = 0; i < 8; ++i) PIN(t2[i]);
        transpose8(u, ta);
        transpose8(u + 8, tb);
#pragma unroll
        for (int i = 0; i < 8; ++i) { PIN(ta[i]); PIN(tb[i]); }
        if (PREY) bst(0.f, 0.f, tag - 1, ws, l16, WS_Y + cq * 65536 + rho * 1024);
        ST(5);
        lds_wait<24>();
        if (GATE_W) lds_wait<36>();
        ST(4);
        u32x4 og[8];
#pragma unroll
        for (int k = 0; k < 8; ++k) og[k] = bld(ws, oga + k * 256, WS_O + rho * 8192);
        SB();
        lds_wait<28>();
        const u32x4 af = *(volatile LAS u32x4*)(lds + aa);
        f32x4 d[8];
#pragma unroll
        for (int e = 0; e < 8; ++e) { d[e] = mfma(af, t2[e], f32x4{0.f, 0.f, 0.f, 0.f}); PIN(d[e]); }
        if (!GATE_W) lds_wait<36>();
        u32x4 wf = *(volatile LAS u32x4*)(lds + aa + (L_W - L_A));
        PIN(wf);
        SB();
        ST(3);
        {
            u32 bad = 0;
#pragma unroll
            for (int k = 0; k < 8; ++k) bad = chk(bad, og[k], tag);
            ST(10);
            if (__builtin_expect(__builtin_amdgcn_ballot_w64(bad != 0) != 0, 0)) {
#pragma nounroll
                for (u32 n = 1; n < SPIN_CAP; ++n) {
                    MEMBAR();
#pragma unroll
                    for (int k = 0; k < 8; ++k) og[k] = bld(ws, oga + k * 256, WS_O + rho * 8192);
                    bad = 0;
#pragma unroll
                    for (int k = 0; k < 8; ++k) bad = chk(bad, og[k], tag);
                    CNT(9, n);
                    if (__builtin_amdgcn_ballot_w64(bad != 0) == 0) break;
                }
            }
        }
        ST(7);
        f32x4 c[4];
#pragma unroll
        for (int e = 0; e < 4; ++e) {
            u32x4 ot;
#pragma unroll
            for (int p = 0; p < 4; ++p) ot[p] = __builtin_amdgcn_perm(og[2 * p + 1][e & 2], og[2 * p][e & 2], (e & 1) ? 0x07060302u : 0x05040100u);
            c[e] = mfma(ot, wf, f32x4{0.f, 0.f, 0.f, 0.f});
        }
        const u32x4 a0 = {cvtpk(c[0][0], c[0][1]), cvtpk(c[0][2], c[0][3]), cvtpk(c[1][0], c[1][1]), cvtpk(c[1][2], c[1][3])};
        const u32x4 a1 = {cvtpk(c[2][0], c[2][1]), cvtpk(c[2][2], c[2][3]), cvtpk(c[3][0], c[3][1]), cvtpk(c[3][2], c[3][3])};
#pragma unroll
        for (int e = 0; e < 8; ++e) d[e] = mfma(a0, ta[e], d[e]);
#pragma unroll
        for (int e = 0; e < 8; ++e) d[e] = mfma(a1, tb[e], d[e]);
        const bool k0 = kg & 1, k1 = kg & 2;
        const float ya0 = k0 ? d[2][0] : d[0][0], yb0 = k0 ? d[6][0] : d[4][0], ya1 = k0 ? d[3][0] : d[1][0], yb1 = k0 ? d[7][0] : d[5][0];
        const float y0 = k1 ? yb0 : ya0, y1 = k1 ? yb1 : ya1;
        bst(y0, y1, tag, ws, l16, WS_Y + cq * 65536 + rho * 1024);
        SB();
        ST(6);
        __builtin_amdgcn_s_sleep(DELAY_Y);
        flag[8] = 1;
        ST_OUT();
    }
}

extern "C" void kernel_launch(void* const* d_in, const int* in_sizes, int n_in, void* d_out, int out_size, void* d_ws, size_t ws_size, hipStream_t stream) {
    Args a;
    a.x = (const char*)d_in[0]; a.k = (const char*)d_in[1]; a.v = (const char*)d_in[2]; a.wq = (const char*)d_in[3]; a.wo = (const char*)d_in[4];
    a.w1 = (const char*)d_in[6]; a.b1 = (const char*)d_in[7]; a.w2 = (const char*)d_in[8]; a.b2 = (const char*)d_in[9]; a.gw = (const char*)d_in[10];
    a.out = (char*)d_out; a.ws = (char*)d_ws + (WSOFF ? ws_size - (8u << 20) : 0);
    gated_mix_kernel<<<256, 256, 0, stream>>>(a);
}
```
